```python
import torch

def _softmax(x, axis=-1):
    return torch.softmax(x, dim=axis)
def _gelu(x):
    return torch.nn.functional.gelu(x, approximate="tanh")
def _silu(x):
    return torch.nn.functional.silu(x)
def _sigmoid(x):
    return torch.sigmoid(x)
def _layernorm(x, axis=-1, eps=1e-5):
    m = x.mean(dim=axis, keepdim=True)
    v = x.var(dim=axis, keepdim=True, unbiased=False)
    return (x - m) / torch.sqrt(v + eps)
def _scatter_add(tgt, idx, src, axis=0):
    return tgt.clone().index_add_(axis, idx, src)
def _segment_sum(src, seg, n):
    out = torch.zeros((n,) + src.shape[1:], dtype=src.dtype)
    return out.index_add_(0, seg, src)
def _pad(x, pw):
    flat = tuple(v for pair in reversed(pw) for v in pair)
    return torch.nn.functional.pad(x, flat)

kernel_name = 'standalone__w4a16_gemm_dequant__prompt'

def setup_inputs():

    _w = (lambda a: (lambda t: t.to(torch.bfloat16) if t.is_floating_point() else t)(torch.as_tensor(a)))
    B, S, D, F = 1, 2048, 1024, 3584
    G = 32 if 32 > 0 else D
    D = (D // G) * G
    ng = D // G
    qw = torch.randint(0, 16, (D, F)).to(torch.int32)
    return {
        "x": _w((lambda *s: torch.randn(*s, dtype=torch.bfloat16) * 0.05)(B, S, D)),
        "qw": _w(qw),
        "scales": _w(torch.abs((lambda *s: torch.randn(*s, dtype=torch.bfloat16) * 0.05)(ng, F)) + 0.01),
        "zeros": _w(torch.full((ng, F), 8.0, dtype=float)),
    }

def reference(x, qw, scales, zeros):
    B, S, D = x.shape
    ng, F = scales.shape
    G = D // ng
    s = (lambda x, n, axis: torch.repeat_interleave(x, n, dim=axis))(scales, G, axis=0)
    z = (lambda x, n, axis: torch.repeat_interleave(x, n, dim=axis))(zeros, G, axis=0)
    w = ((lambda x, dt: x.to(dt))(qw, x.dtype) - z) * s
    return x @ w
```

```cpp
#include <hip/hip_runtime.h>
#include <hip/hip_bf16.h>
#include <stdint.h>

using bf16x8 = __attribute__((ext_vector_type(8))) __bf16;
using f32x4 = __attribute__((ext_vector_type(4))) float;
constexpr int M = 2048, K = 1024, N = 3584, G = 32;

__device__ __forceinline__ float bf2f(uint16_t v) { return __uint_as_float((uint32_t)v << 16); }

__global__ __launch_bounds__(256) void naive_kernel(const uint16_t* __restrict__ x, const int* __restrict__ qw,
                                                    const uint16_t* __restrict__ sc, const uint16_t* __restrict__ zr,
                                                    uint16_t* __restrict__ out) {
  int lane = threadIdx.x & 63, w = threadIdx.x >> 6;
  int n0 = (blockIdx.x * 4 + w) * 16, m0 = blockIdx.y * 64;
  int c = lane & 15, q = lane >> 4;
  f32x4 acc[4] = {};
  for (int k0 = 0; k0 < K; k0 += 32) {
    float s = bf2f(sc[(k0 / G) * N + n0 + c]), z = bf2f(zr[(k0 / G) * N + n0 + c]);
    bf16x8 b;
    for (int j = 0; j < 8; ++j) b[j] = (__bf16)(((float)qw[(k0 + q * 8 + j) * N + n0 + c] - z) * s);
    for (int t = 0; t < 4; ++t) {
      bf16x8 a = *(const bf16x8*)(x + (m0 + t * 16 + c) * K + k0 + q * 8);
      acc[t] = __builtin_amdgcn_mfma_f32_16x16x32_bf16(a, b, acc[t], 0, 0, 0);
    }
  }
  for (int t = 0; t < 4; ++t)
    for (int j = 0; j < 4; ++j) {
      __bf16 v = (__bf16)acc[t][j];
      out[(m0 + t * 16 + q * 4 + j) * N + n0 + c] = *(uint16_t*)&v;
    }
}

extern "C" void kernel_launch(void* const* d_in, const int* in_sizes, int n_in, void* d_out, int out_size,
                              void* d_ws, size_t ws_size, hipStream_t stream) {
  naive_kernel<<<dim3(N / 64, M / 64), 256, 0, stream>>>((const uint16_t*)d_in[0], (const int*)d_in[1],
                                                         (const uint16_t*)d_in[2], (const uint16_t*)d_in[3],
                                                         (uint16_t*)d_out);
}
```

```cpp
#include <hip/hip_runtime.h>
#include <stdint.h>
typedef unsigned long long u64;
__global__ __launch_bounds__(512) __attribute__((aligned(4096))) void w4a16_kernel(const void* sc, const void* zr, void* out, const void* x, void* ws, const void* qw) {
  __shared__ __attribute__((aligned(1024))) char lds[154624];


  asm volatile(
    "s_load_dwordx2 s[12:13], %[ka], 40\n"
    "s_load_dwordx2 s[28:29], %[ka], 32\n"
    "s_load_dwordx2 s[8:9], %[ka], 24\n"
    "s_load_dwordx2 s[16:17], %[ka], 0\n"
    "s_load_dwordx2 s[20:21], %[ka], 8\n"
    "s_load_dwordx2 s[24:25], %[ka], 16\n"
    "v_readfirstlane_b32 s67, %[tid]\n"
    "s_getpc_b64 s[70:71]\n"
    "3:\n"
    "s_bitcmp0_b32 s67, 8\n"
    "s_cbranch_scc1 4f\n"
    "v_and_b32 v9, 0xff, %[tid]\n"
    "v_lshlrev_b32 v9, 7, v9\n"
    "v_min_u32 v9, 99f-3b-4, v9\n"
    "global_load_dword v234, v9, s[70:71]\n"
    "s_branch 40f\n"
    "4:\n"
    "s_setprio 1\n"
    "s_lshr_b32 s42, %[b], 3\n"
    "s_bfe_u32 s43, %[b], 0x20001\n"
    "s_mul_i32 s44, s43, 63\n"
    "s_add_u32 s44, s44, 1\n"
    "s_lshr_b32 s44, s44, 1\n"
    "s_add_u32 s44, s44, s42\n"
    "s_and_b32 s43, s43, 1\n"
    "s_add_u32 s43, s43, s42\n"
    "s_cmp_lg_u32 s43, 32\n"
    "s_cbranch_scc1 1f\n"
    "s_endpgm\n"
    "1:\n"
    "s_add_u32 s68, s43, s42\n"
    "s_mul_i32 s68, s68, 9363\n"
    "s_lshr_b32 s68, s68, 16\n"
    "s_mul_i32 s34, s44, 4682\n"
    "s_lshr_b32 s34, s34, 16\n"
    "s_mul_i32 s43, s34, 14\n"
    "s_sub_u32 s33, s44, s43\n"
    "s_and_b32 s43, %[b], 1\n"
    "s_mul_i32 s43, s43, 14\n"
    "s_add_u32 s33, s33, s43\n"
    "s_bfe_u32 s66, s67, 0x20006\n"
    "s_mov_b32 s14, 14680064\n"
    "s_mov_b32 s15, 0x20000\n"
    "s_lshl3_add_u32 s42, s68, s66\n"
    "s_mul_i32 s63, s42, 57344\n"
    "s_lshl_b32 s43, s33, 9\n"
    "s_add_u32 s63, s63, s43\n"
    "v_and_b32 v2, 31, %[tid]\n"
    "v_bfe_u32 v3, %[tid], 5, 1\n"
    "v_mul_u32_u24 v4, 28672, v3\n"
    "v_lshl_add_u32 v5, v2, 4, v4\n"
    "s_lshl_b32 s65, s33, 7\n"
    "s_add_u32 s65, s65, 131072\n"
    "s_waitcnt lgkmcnt(0)\n"
    "s_load_dword s39, s[28:29], s65\n"
    "buffer_load_dwordx4 v[52:55], v5, s[12:15], s63 offen\n"
    "s_add_u32 s43, s63, 14336\n"
    "buffer_load_dwordx4 v[56:59], v5, s[12:15], s43 offen\n"
    "s_add_u32 s63, s63, 229376\n"
    "buffer_load_dwordx4 v[60:63], v5, s[12:15], s63 offen\n"
    "s_add_u32 s43, s63, 14336\n"
    "buffer_load_dwordx4 v[64:67], v5, s[12:15], s43 offen\n"
    "s_mov_b32 s44, 0x22211110\n"
    "s_mov_b32 s45, 0x44333322\n"
    "s_and_b32 s42, s33, 3\n"
    "s_lshl_b32 s42, s42, 3\n"
    "s_lshr_b64 s[44:45], s[44:45], s42\n"
    "s_and_b32 s43, s44, 15\n"
    "s_mul_i32 s35, s34, 14\n"
    "s_sub_u32 s35, s35, s43\n"
    "s_lshl_b32 s42, s34, 2\n"
    "s_lshr_b64 s[44:45], s[44:45], s42\n"
    "s_and_b32 s43, s44, 15\n"
    "s_add_u32 s35, s35, s43\n"
    "s_bfe_u32 s36, s44, 0x40004\n"
    "s_add_u32 s36, s36, 14\n"
    "s_sub_u32 s36, s36, s43\n"
    "v_and_b32 v7, 63, %[tid]\n"
    "v_lshlrev_b32 v7, 4, v7\n"
    "v_lshrrev_b32 v18, 7, v7\n"
    "v_lshrrev_b32 v19, 4, v7\n"
    "v_xor_b32 v19, v19, v7\n"
    "v_and_b32 v19, 0x70, v19\n"
    "v_lshl_or_b32 v18, v18, 11, v19\n"
    "s_mul_i32 s42, s66, 7\n"
    "s_lshl_b32 s57, s42, 10\n"
    "s_and_b32 s43, s42, 1\n"
    "s_lshl_b32 s43, s43, 6\n"
    "s_lshl1_add_u32 s42, s35, s42\n"
    "s_lshl_b32 s42, s42, 14\n"
    "s_or_b32 s42, s42, s43\n"
    "v_xor_b32 v8, s42, v18\n"
    "v_xor_b32 v19, 64, v8\n"
    "v_add_u32 v9, 15360, v19\n"
    "v_add_u32 v10, 30720, v8\n"
    "v_add_u32 v11, 46080, v19\n"
    "v_add_u32 v12, 65536, v8\n"
    "v_add_u32 v13, 80896, v19\n"
    "v_add_u32 v14, 96256, v8\n"
    "s_and_b32 s43, s66, 1\n"
    "s_lshl_b32 s43, s43, 6\n"
    "s_lshl1_add_u32 s42, s35, s66\n"
    "s_add_u32 s42, s42, 28\n"
    "s_lshl_b32 s42, s42, 14\n"
    "s_or_b32 s42, s42, s43\n"
    "v_xor_b32 v15, s42, v18\n"
    "s_add_u32 s59, s66, 28\n"
    "s_lshl_b32 s42, s36, 1\n"
    "s_sub_u32 s61, s42, s59\n"
    "s_lshl_b32 s59, s59, 10\n"
    "s_sub_u32 s59, s59, s57\n"
    "s_mov_b32 s10, 4194304\n"
    "s_mov_b32 s11, 0x20000\n"
    "s_mov_b32 s53, 0\n"
    "s_mov_b32 s54, s57\n"
    "s_call_b64 s[48:49], 92f\n"
    "s_branch 51f\n"
    "91:\n"
    "s_add_u32 s53, s53, 128\n"
    "s_add_u32 s54, s54, 38912\n"
    "92:\n"
    "s_add_u32 m0, s54, 0\n"
    "s_add_u32 s42, s54, s59\n"
    "buffer_load_dwordx4 v8, s[8:11], s53 offen offset:0 lds\n"
    "buffer_load_dwordx4 v9, s[8:11], s53 offen offset:1024 lds\n"
    "buffer_load_dwordx4 v10, s[8:11], s53 offen offset:2048 lds\n"
    "buffer_load_dwordx4 v11, s[8:11], s53 offen offset:3072 lds\n"
    "s_add_u32 m0, s54, 4096\n"
    "s_add_u32 s42, s54, s59\n"
    "buffer_load_dwordx4 v12, s[8:11], s53 offen offset:0 lds\n"
    "buffer_load_dwordx4 v13, s[8:11], s53 offen offset:1024 lds\n"
    "buffer_load_dwordx4 v14, s[8:11], s53 offen offset:2048 lds\n"
    "s_add_u32 m0, s42, 0\n"
    "s_cmp_gt_i32 s61, 0\n"
    "s_cbranch_scc0 59f\n"
    "buffer_load_dwordx4 v15, s[8:11], s53 offen lds\n"
    "59:\n"
    "s_setpc_b64 s[48:49]\n"
    "51:\n"
    "s_mov_b32 s30, 3932160\n"
    "s_mov_b32 s31, 0x20000\n"
    "s_lshl_b32 s62, s33, 17\n"
    "s_add_u32 s62, s62, 262144\n"
    "s_mov_b32 s50, 0x0ff00ff0\n"
    "v_mul_u32_u24 v3, 0x140, v3\n"
    "v_lshlrev_b32 v2, 3, v2\n"
    "v_xor_b32 v6, v2, v3\n"
    "s_lshl3_add_u32 s42, s68, s66\n"
    "s_lshl_b32 s64, s42, 9\n"
    "s_add_u32 s64, s64, s62\n"
    "s_waitcnt lgkmcnt(0)\n"
    "s_add_u32 s39, s39, 16\n"
    "s_and_b32 s40, s39, s50\n"
    "s_lshr_b32 s42, s39, 8\n"
    "s_lshl_b32 s43, s39, 24\n"
    "s_or_b32 s42, s42, s43\n"
    "s_and_b32 s41, s42, s50\n"
    "s_waitcnt vmcnt(9)\n"
    "v_lshl_or_b32 v84, v53, 12, v52\n"
    "v_lshl_or_b32 v108, v57, 12, v56\n"
    "v_lshl_or_b32 v85, v55, 12, v54\n"
    "v_lshl_or_b32 v109, v59, 12, v58\n"
    "v_lshl_or_b32 v84, v108, 16, v84\n"
    "v_lshl_or_b32 v85, v109, 16, v85\n"
    "s_add_u32 s64, s64, 0\n"
    "s_nop 0\n"
    "v_permlane32_swap_b32 v84, v85\n"
    "v_or_b32 v84, s40, v84\n"
    "v_or_b32 v85, s41, v85\n"
    "buffer_store_dwordx2 v[84:85], v6, s[28:31], s64 offen sc1\n"
    "s_waitcnt vmcnt(8)\n"
    "v_lshl_or_b32 v86, v61, 12, v60\n"
    "v_lshl_or_b32 v108, v65, 12, v64\n"
    "v_lshl_or_b32 v87, v63, 12, v62\n"
    "v_lshl_or_b32 v109, v67, 12, v66\n"
    "v_lshl_or_b32 v86, v108, 16, v86\n"
    "v_lshl_or_b32 v87, v109, 16, v87\n"
    "s_add_u32 s64, s64, 2048\n"
    "s_nop 0\n"
    "v_permlane32_swap_b32 v86, v87\n"
    "v_or_b32 v86, s40, v86\n"
    "v_or_b32 v87, s41, v87\n"
    "buffer_store_dwordx2 v[86:87], v6, s[28:31], s64 offen sc1\n"
    "s_add_u32 s58, s57, 116736\n"
    "s_lshl_b32 s42, s66, 11\n"
    "s_add_u32 s46, s42, 30720\n"
    "s_sub_u32 s60, s46, s57\n"
    "s_add_u32 s55, s42, 4096\n"
    "v_add_u32 v16, s62, v7\n"
    "s_mov_b32 s47, s55\n"
    "s_mov_b32 s56, 14\n"
    "s_mov_b32 s52, 200000\n"
    "s_lshl_b32 s45, s66, 10\n"
    "s_add_u32 s44, s45, 149504\n"
    "v_add_u32 v18, s44, v7\n"
    "v_add_u32 v17, s46, v7\n"
    "s_cmp_eq_u32 s68, 0\n"
    "s_cselect_b32 s69, 32, 0\n"
    "s_or_b32 s69, s69, 0xaa80\n"
    "s_lshr_b32 s38, s69, 2\n"
    "s_lshl2_add_u32 s42, s68, s66\n"
    "s_add_u32 s42, s42, 36\n"
    "s_mul_i32 s63, s42, 57344\n"
    "s_lshl_b32 s43, s33, 9\n"
    "s_add_u32 s63, s63, s43\n"
    "s_lshl2_add_u32 s42, s68, s66\n"
    "s_add_u32 s42, s42, 36\n"
    "s_lshl_b32 s64, s42, 9\n"
    "s_add_u32 s64, s64, s62\n"
    "s_add_u32 s70, s70, 97f-3b\n"
    "s_addc_u32 s71, s71, 0\n"
    "s_mov_b32 s73, s71\n"
    "s_add_u32 s42, s61, 3\n"
    "s_ashr_i32 s42, s42, 2\n"
    "s_max_i32 s42, s42, 0\n"
    "s_add_u32 s42, s42, 9\n"
    "s_lshl3_add_u32 s37, s42, s70\n"
    "s_add_u32 s70, s70, 40\n"
    "s_waitcnt vmcnt(0)\n"
    "65:\n"
    "s_mov_b32 m0, s44\n"
    "s_sub_u32 s52, s52, 1\n"
    "buffer_load_dwordx4 v16, s[28:31], s45 offen sc1 lds\n"
    "s_mov_b32 m0, s46\n"
    "s_cselect_b32 s52, 0, s52\n"
    "buffer_load_dwordx4 v16, s[28:31], s47 offen offset:0 sc1 lds\n"
    "buffer_load_dwordx4 v16, s[28:31], s47 offen offset:1024 sc1 lds\n"
    "s_call_b64 s[48:49], 91b\n"
    "s_barrier\n"
    "s_add_u32 s63, s63, 2064384\n"
    "buffer_load_dwordx4 v[52:55], v5, s[12:15], s63 offen\n"
    "s_add_u32 s43, s63, 14336\n"
    "buffer_load_dwordx4 v[56:59], v5, s[12:15], s43 offen\n"
    "s_waitcnt vmcnt(9)\n"
    "61:\n"
    "ds_read_b128 v[20:23], v17\n"
    "ds_read_b128 v[24:27], v17 offset:1024\n"
    "ds_read_b128 v[32:35], v18\n"
    "s_cmp_eq_u32 s52, 0\n"
    "s_waitcnt lgkmcnt(0)\n"
    "v_bitop3_b32 v36, v20, v22, s40 bitop3:0x7e\n"
    "v_bitop3_b32 v37, v21, v23, s41 bitop3:0x7e\n"
    "v_bitop3_b32 v38, v24, v26, s40 bitop3:0x7e\n"
    "v_bitop3_b32 v39, v25, v27, s41 bitop3:0x7e\n"
    "v_bitop3_b32 v40, v32, v34, s40 bitop3:0x7e\n"
    "v_bitop3_b32 v41, v33, v35, s41 bitop3:0x7e\n"
    "v_or3_b32 v36, v36, v37, v38\n"
    "v_or3_b32 v39, v39, v40, v41\n"
    "v_bitop3_b32 v36, v36, v39, s50 bitop3:0xa8\n"
    "v_cmp_ne_u32 vcc, 0, v36\n"
    "s_cbranch_scc1 62f\n"
    "s_cbranch_vccz 62f\n"
    "s_mov_b32 m0, s44\n"
    "s_sub_u32 s52, s52, 1\n"
    "buffer_load_dwordx4 v16, s[28:31], s45 offen sc1 lds\n"
    "s_mov_b32 m0, s46\n"
    "s_cselect_b32 s52, 0, s52\n"
    "buffer_load_dwordx4 v16, s[28:31], s47 offen offset:0 sc1 lds\n"
    "buffer_load_dwordx4 v16, s[28:31], s47 offen offset:1024 sc1 lds\n"
    "s_waitcnt vmcnt(0)\n"
    "s_branch 61b\n"
    "62:\n"
    "s_barrier\n"
    "s_memrealtime s[16:17]\n"
    "s_mov_b32 s20, 76\n"
    "s_add_u32 m0, s54, s60\n"
    "s_add_u32 s55, s55, 8192\n"
    "s_and_b32 s55, s55, 0x1ffff\n"
    "buffer_load_dwordx4 v16, s[28:31], s55 offen offset:0 sc1 lds\n"
    "buffer_load_dwordx4 v16, s[28:31], s55 offen offset:1024 sc1 lds\n"
    "52:\n"
    "s_add_u32 s46, s54, s60\n"
    "s_mov_b32 s47, s55\n"
    "s_cmp_eq_u32 s56, 0\n"
    "v_add_u32 v17, s46, v7\n"
    "s_cbranch_scc1 53f\n"
    "s_add_u32 s54, s54, 38912\n"
    "s_cmp_eq_u32 s54, s58\n"
    "s_cselect_b32 s54, s57, s54\n"
    "s_add_u32 m0, s54, s60\n"
    "s_add_u32 s55, s55, 8192\n"
    "s_and_b32 s55, s55, 0x1ffff\n"
    "buffer_load_dwordx4 v16, s[28:31], s55 offen offset:0 sc1 lds\n"
    "buffer_load_dwordx4 v16, s[28:31], s55 offen offset:1024 sc1 lds\n"
    "s_add_u32 s53, s53, 128\n"
    "s_mov_b32 s72, s37\n"
    "s_bitcmp0_b32 s38, s56\n"
    "s_cbranch_scc1 63f\n"
    "v_lshl_or_b32 v84, v53, 12, v52\n"
    "v_lshl_or_b32 v108, v57, 12, v56\n"
    "v_lshl_or_b32 v85, v55, 12, v54\n"
    "v_lshl_or_b32 v109, v59, 12, v58\n"
    "v_lshl_or_b32 v84, v108, 16, v84\n"
    "v_lshl_or_b32 v85, v109, 16, v85\n"
    "s_add_u32 s64, s64, 18432\n"
    "s_add_u32 s70, s70, 8\n"
    "v_permlane32_swap_b32 v84, v85\n"
    "v_or_b32 v84, s40, v84\n"
    "v_or_b32 v85, s41, v85\n"
    "buffer_store_dwordx2 v[84:85], v6, s[28:31], s64 offen sc1\n"
    "s_add_u32 s72, s72, 8\n"
    "63:\n"
    "s_bitcmp0_b32 s69, s56\n"
    "s_cbranch_scc1 64f\n"
    "s_add_u32 s63, s63, 2064384\n"
    "buffer_load_dwordx4 v[52:55], v5, s[12:15], s63 offen\n"
    "s_add_u32 s43, s63, 14336\n"
    "buffer_load_dwordx4 v[56:59], v5, s[12:15], s43 offen\n"
    "s_add_u32 s70, s70, 16\n"
    "s_add_u32 s72, s72, 16\n"
    "64:\n"
    "s_add_u32 m0, s54, 0\n"
    "s_add_u32 s42, s54, s59\n"
    "buffer_load_dwordx4 v8, s[8:11], s53 offen offset:0 lds\n"
    "buffer_load_dwordx4 v9, s[8:11], s53 offen offset:1024 lds\n"
    "buffer_load_dwordx4 v10, s[8:11], s53 offen offset:2048 lds\n"
    "s_swappc_b64 s[48:49], s[70:71]\n"
    "ds_read_b128 v[20:23], v17\n"
    "ds_read_b128 v[24:27], v17 offset:1024\n"
    "s_memrealtime s[18:19]\n"
    "buffer_load_dwordx4 v11, s[8:11], s53 offen offset:3072 lds\n"
    "s_add_u32 m0, s54, 4096\n"
    "s_add_u32 s42, s54, s59\n"
    "buffer_load_dwordx4 v12, s[8:11], s53 offen offset:0 lds\n"
    "s_waitcnt lgkmcnt(0)\n"
    "v_bitop3_b32 v28, v20, v22, s40 bitop3:0x7e\n"
    "v_bitop3_b32 v29, v21, v23, s41 bitop3:0x7e\n"
    "v_bitop3_b32 v30, v24, v26, s40 bitop3:0x7e\n"
    "v_bitop3_b32 v31, v25, v27, s41 bitop3:0x7e\n"
    "v_or3_b32 v28, v28, v29, v30\n"
    "v_bitop3_b32 v28, v28, v31, s50 bitop3:0xa8\n"
    "v_cmp_ne_u32 vcc, 0, v28\n"
    "buffer_load_dwordx4 v13, s[8:11], s53 offen offset:1024 lds\n"
    "buffer_load_dwordx4 v14, s[8:11], s53 offen offset:2048 lds\n"
    "s_add_u32 m0, s42, 0\n"
    "s_cmp_gt_i32 s61, 0\n"
    "s_cbranch_scc0 56f\n"
    "buffer_load_dwordx4 v15, s[8:11], s53 offen lds\n"
    "56:\n"
    "s_add_u32 s70, s72, 24\n"
    "s_cbranch_vccnz 94f\n"
    "s_swappc_b64 s[48:49], s[72:73]\n"
    "55:\n"
    "s_cmp_ge_u32 s56, 12\n"
    "s_cselect_b32 s42, 0, 99\n"
    "s_sub_u32 s20, s20, s42\n"
    "s_max_i32 s20, s20, 0\n"
    "s_add_u32 s16, s16, s20\n"
    "71:\n"
    "s_sub_u32 s42, s16, s18\n"
    "s_sub_u32 s42, s42, 11\n"
    "s_cmp_lt_u32 s42, 1216\n"
    "s_cbranch_scc0 72f\n"
    "s_memrealtime s[18:19]\n"
    "s_waitcnt lgkmcnt(0)\n"
    "s_branch 71b\n"
    "72:\n"
    "s_barrier\n"
    "s_sub_u32 s56, s56, 1\n"
    "s_cbranch_scc0 52b\n"
    "s_or_b32 s42, s34, s66\n"
    "s_cbranch_scc1 57f\n"
    "v_mov_b32 v8, s39\n"
    "buffer_store_dword v8, off, s[28:31], s65 sc1\n"
    "57:\n"
    "v_lshrrev_b32 v8, 8, v7\n"
    "s_lshl_b32 s42, s66, 2\n"
    "v_add_u32 v8, s42, v8\n"
    "v_and_b32 v9, 0xf0, v7\n"
    "s_mov_b32 s43, 7168\n"
    "v_mad_u32_u24 v18, v8, s43, v9\n"
    "v_mul_u32_u24 v8, 0x110, v8\n"
    "v_xor_b32 v19, v8, v9\n"
    "v_add_u32 v20, 30720, v19\n"
    "v_add_u32 v19, 108544, v19\n"
    "s_mul_i32 s42, s35, 114688\n"
    "s_lshl_b32 s43, s33, 8\n"
    "s_add_u32 s42, s42, s43\n"
    "s_mov_b32 s26, 14680064\n"
    "s_mov_b32 s27, 0x20000\n"
    "s_barrier\n"
    "ds_read_b128 v[52:55], v19 offset:8192\n"
    "ds_read_b128 v[56:59], v19 offset:12288\n"
    "s_waitcnt lgkmcnt(1)\n"
    "buffer_store_dwordx4 v[52:55], v18, s[24:27], s42 offen sc1\n"
    "s_add_u32 s42, s42, 114688\n"
    "s_waitcnt lgkmcnt(0)\n"
    "buffer_store_dwordx4 v[56:59], v18, s[24:27], s42 offen sc1\n"
    "s_add_u32 s42, s42, 114688\n"
    "s_barrier\n"
    "ds_read_b128 v[60:63], v19 offset:16384\n"
    "ds_read_b128 v[64:67], v19 offset:20480\n"
    "ds_read_b128 v[68:71], v19 offset:24576\n"
    "ds_read_b128 v[72:75], v19 offset:28672\n"
    "s_waitcnt lgkmcnt(3)\n"
    "buffer_store_dwordx4 v[60:63], v18, s[24:27], s42 offen sc1\n"
    "s_add_u32 s42, s42, 114688\n"
    "s_waitcnt lgkmcnt(2)\n"
    "buffer_store_dwordx4 v[64:67], v18, s[24:27], s42 offen sc1\n"
    "s_add_u32 s42, s42, 114688\n"
    "s_waitcnt lgkmcnt(1)\n"
    "buffer_store_dwordx4 v[68:71], v18, s[24:27], s42 offen sc1\n"
    "s_add_u32 s42, s42, 114688\n"
    "s_waitcnt lgkmcnt(0)\n"
    "buffer_store_dwordx4 v[72:75], v18, s[24:27], s42 offen sc1\n"
    "s_add_u32 s42, s42, 114688\n"
    "s_barrier\n"
    "ds_read_b128 v[76:79], v19 offset:40960\n"
    "ds_read_b128 v[80:83], v19 offset:32768\n"
    "ds_read_b128 v[84:87], v19 offset:36864\n"
    "ds_read_b128 v[88:91], v19 offset:0\n"
    "s_waitcnt lgkmcnt(3)\n"
    "buffer_store_dwordx4 v[76:79], v18, s[24:27], s42 offen sc1\n"
    "s_add_u32 s42, s42, 114688\n"
    "s_waitcnt lgkmcnt(2)\n"
    "buffer_store_dwordx4 v[80:83], v18, s[24:27], s42 offen sc1\n"
    "s_add_u32 s42, s42, 114688\n"
    "s_waitcnt lgkmcnt(1)\n"
    "buffer_store_dwordx4 v[84:87], v18, s[24:27], s42 offen sc1\n"
    "s_add_u32 s42, s42, 114688\n"
    "s_waitcnt lgkmcnt(0)\n"
    "buffer_store_dwordx4 v[88:91], v18, s[24:27], s42 offen sc1\n"
    "s_add_u32 s42, s42, 114688\n"
    "s_barrier\n"
    "ds_read_b128 v[92:95], v19 offset:4096\n"
    "ds_read_b128 v[96:99], v20 offset:38912\n"
    "ds_read_b128 v[100:103], v20 offset:43008\n"
    "ds_read_b128 v[104:107], v20 offset:0\n"
    "ds_read_b128 v[108:111], v20 offset:4096\n"
    "s_waitcnt lgkmcnt(4)\n"
    "buffer_store_dwordx4 v[92:95], v18, s[24:27], s42 offen sc1\n"
    "s_add_u32 s42, s42, 114688\n"
    "s_waitcnt lgkmcnt(3)\n"
    "buffer_store_dwordx4 v[96:99], v18, s[24:27], s42 offen sc1\n"
    "s_add_u32 s42, s42, 114688\n"
    "s_waitcnt lgkmcnt(2)\n"
    "buffer_store_dwordx4 v[100:103], v18, s[24:27], s42 offen sc1\n"
    "s_add_u32 s42, s42, 114688\n"
    "s_waitcnt lgkmcnt(1)\n"
    "buffer_store_dwordx4 v[104:107], v18, s[24:27], s42 offen sc1\n"
    "s_add_u32 s42, s42, 114688\n"
    "s_cmp_le_u32 s36, 14\n"
    "s_cbranch_scc1 58f\n"
    "s_waitcnt lgkmcnt(0)\n"
    "buffer_store_dwordx4 v[108:111], v18, s[24:27], s42 offen sc1\n"
    "s_add_u32 s42, s42, 114688\n"
    "58:\n"
    "s_endpgm\n"
    "94:\n"
    "s_mov_b32 m0, s46\n"
    "s_nop 0\n"
    "buffer_load_dwordx4 v16, s[28:31], s47 offen offset:0 sc1 lds\n"
    "buffer_load_dwordx4 v16, s[28:31], s47 offen offset:1024 sc1 lds\n"
    "53:\n"
    "s_waitcnt vmcnt(0)\n"
    "ds_read_b128 v[20:23], v17\n"
    "ds_read_b128 v[24:27], v17 offset:1024\n"
    "s_sub_u32 s52, s52, 1\n"
    "s_waitcnt lgkmcnt(0)\n"
    "v_bitop3_b32 v28, v20, v22, s40 bitop3:0x7e\n"
    "v_bitop3_b32 v29, v21, v23, s41 bitop3:0x7e\n"
    "v_bitop3_b32 v30, v24, v26, s40 bitop3:0x7e\n"
    "v_bitop3_b32 v31, v25, v27, s41 bitop3:0x7e\n"
    "v_or3_b32 v28, v28, v29, v30\n"
    "v_bitop3_b32 v28, v28, v31, s50 bitop3:0xa8\n"
    "v_cmp_ne_u32 vcc, 0, v28\n"
    "s_cselect_b32 s52, 0, s52\n"
    "s_cbranch_scc1 55b\n"
    "s_cbranch_vccnz 94b\n"
    "s_branch 55b\n"
    ".p2align 8\n"
    "97:\n"
    "s_waitcnt vmcnt(0)\n"
    "s_setpc_b64 s[48:49]\n"
    "s_waitcnt vmcnt(1)\n"
    "s_setpc_b64 s[48:49]\n"
    "s_waitcnt vmcnt(2)\n"
    "s_setpc_b64 s[48:49]\n"
    "s_waitcnt vmcnt(3)\n"
    "s_setpc_b64 s[48:49]\n"
    "s_waitcnt vmcnt(4)\n"
    "s_setpc_b64 s[48:49]\n"
    "s_waitcnt vmcnt(5)\n"
    "s_setpc_b64 s[48:49]\n"
    "s_waitcnt vmcnt(6)\n"
    "s_setpc_b64 s[48:49]\n"
    "95:\n"
    "s_waitcnt vmcnt(7)\n"
    "s_setpc_b64 s[48:49]\n"
    "s_waitcnt vmcnt(8)\n"
    "s_setpc_b64 s[48:49]\n"
    "s_waitcnt vmcnt(9)\n"
    "s_setpc_b64 s[48:49]\n"
    "s_waitcnt vmcnt(10)\n"
    "s_setpc_b64 s[48:49]\n"
    "s_waitcnt vmcnt(11)\n"
    "s_setpc_b64 s[48:49]\n"
    "s_waitcnt vmcnt(12)\n"
    "s_setpc_b64 s[48:49]\n"
    "s_waitcnt vmcnt(13)\n"
    "s_setpc_b64 s[48:49]\n"
    "s_waitcnt vmcnt(14)\n"
    "s_setpc_b64 s[48:49]\n"
    "s_waitcnt vmcnt(15)\n"
    "s_setpc_b64 s[48:49]\n"
    "s_waitcnt vmcnt(16)\n"
    "s_setpc_b64 s[48:49]\n"
    "s_waitcnt vmcnt(17)\n"
    "s_setpc_b64 s[48:49]\n"
    "s_waitcnt vmcnt(18)\n"
    "s_setpc_b64 s[48:49]\n"
    "s_waitcnt vmcnt(19)\n"
    "s_setpc_b64 s[48:49]\n"
    "s_waitcnt vmcnt(20)\n"
    "s_setpc_b64 s[48:49]\n"
    "s_waitcnt vmcnt(21)\n"
    "s_setpc_b64 s[48:49]\n"
    "s_waitcnt vmcnt(22)\n"
    "s_setpc_b64 s[48:49]\n"
    "96:\n"
    "s_waitcnt vmcnt(23)\n"
    "s_setpc_b64 s[48:49]\n"
    "s_waitcnt vmcnt(24)\n"
    "s_setpc_b64 s[48:49]\n"
    "s_waitcnt vmcnt(25)\n"
    "s_setpc_b64 s[48:49]\n"
    "s_waitcnt vmcnt(26)\n"
    "s_setpc_b64 s[48:49]\n"
    "s_waitcnt vmcnt(27)\n"
    "s_setpc_b64 s[48:49]\n"
    "s_waitcnt vmcnt(28)\n"
    "s_setpc_b64 s[48:49]\n"
    "s_waitcnt vmcnt(29)\n"
    "s_setpc_b64 s[48:49]\n"
    "s_waitcnt vmcnt(30)\n"
    "s_setpc_b64 s[48:49]\n"
    "s_waitcnt vmcnt(31)\n"
    "s_setpc_b64 s[48:49]\n"
    "40:\n"
    "s_lshr_b32 s42, %[b], 3\n"
    "s_bfe_u32 s43, %[b], 0x20001\n"
    "s_mul_i32 s44, s43, 63\n"
    "s_add_u32 s44, s44, 1\n"
    "s_lshr_b32 s44, s44, 1\n"
    "s_add_u32 s44, s44, s42\n"
    "s_and_b32 s43, s43, 1\n"
    "s_add_u32 s43, s43, s42\n"
    "s_cmp_lg_u32 s43, 32\n"
    "s_cbranch_scc1 1f\n"
    "s_endpgm\n"
    "1:\n"
    "s_add_u32 s68, s43, s42\n"
    "s_mul_i32 s68, s68, 9363\n"
    "s_lshr_b32 s68, s68, 16\n"
    "s_mul_i32 s34, s44, 4682\n"
    "s_lshr_b32 s34, s34, 16\n"
    "s_mul_i32 s43, s34, 14\n"
    "s_sub_u32 s33, s44, s43\n"
    "s_and_b32 s43, %[b], 1\n"
    "s_mul_i32 s43, s43, 14\n"
    "s_add_u32 s33, s33, s43\n"
    "s_bfe_u32 s66, s67, 0x20006\n"
    "s_mov_b32 s14, 14680064\n"
    "s_mov_b32 s15, 0x20000\n"
    "v_and_b32 v2, 31, %[tid]\n"
    "v_bfe_u32 v3, %[tid], 5, 1\n"
    "v_mul_u32_u24 v4, 28672, v3\n"
    "v_lshl_add_u32 v5, v2, 4, v4\n"
    "s_lshl_b32 s65, s33, 7\n"
    "s_add_u32 s65, s65, 131072\n"
    "v_and_b32 v7, 63, %[tid]\n"
    "v_lshlrev_b32 v7, 4, v7\n"
    "v_lshrrev_b32 v9, 8, v7\n"
    "v_and_b32 v8, 0xf0, v7\n"
    "s_lshl_b32 s46, s66, 2\n"
    "v_add_u32 v12, s46, v9\n"
    "s_mov_b32 s42, 7168\n"
    "v_mad_u32_u24 v10, v12, s42, v8\n"
    "s_lshl_b32 s42, s33, 8\n"
    "s_add_u32 s43, s42, 114688\n"
    "s_mov_b32 s18, 229376\n"
    "s_mov_b32 s19, 0x20000\n"
    "s_mov_b32 s22, 229376\n"
    "s_mov_b32 s23, 0x20000\n"
    "s_waitcnt lgkmcnt(0)\n"
    "buffer_load_dwordx4 v[92:95], v10, s[16:19], s42 offen\n"
    "buffer_load_dwordx4 v[96:99], v10, s[20:23], s42 offen\n"
    "buffer_load_dwordx4 v[100:103], v10, s[16:19], s43 offen\n"
    "buffer_load_dwordx4 v[104:107], v10, s[20:23], s43 offen\n"
    "s_mov_b32 s44, 0x22211110\n"
    "s_mov_b32 s45, 0x44333322\n"
    "s_and_b32 s42, s33, 3\n"
    "s_lshl_b32 s42, s42, 3\n"
    "s_lshr_b64 s[44:45], s[44:45], s42\n"
    "s_lshl_b32 s42, s34, 2\n"
    "s_lshr_b64 s[44:45], s[44:45], s42\n"
    "s_and_b32 s43, s44, 15\n"
    "s_bfe_u32 s36, s44, 0x40004\n"
    "s_add_u32 s36, s36, 14\n"
    "s_sub_u32 s36, s36, s43\n"
    "v_lshl_or_b32 v16, v12, 10, v8\n"
    "v_add_u32 v16, 116736, v16\n"
    "v_xor_b32 v17, 64, v16\n"
    "v_xor_b32 v18, 128, v16\n"
    "v_xor_b32 v19, 192, v16\n"
    "v_lshlrev_b32 v10, 4, v12\n"
    "v_xor_b32 v10, v10, v8\n"
    "v_lshl_add_u32 v10, v8, 4, v10\n"
    "v_add_u32 v232, 108544, v10\n"
    "v_add_u32 v233, 30720, v10\n"
    "v_lshrrev_b32 v10, 6, v8\n"
    "v_add_lshl_u32 v10, v10, s46, 4\n"
    "v_bfe_u32 v11, v8, 4, 2\n"
    "v_mul_u32_u24 v11, 0x140, v11\n"
    "v_xor_b32 v10, v10, v11\n"
    "v_add_u32 v4, 116736, v10\n"
    "v_lshrrev_b32 v10, 5, v8\n"
    "v_xor_b32 v11, v9, v10\n"
    "v_lshlrev_b32 v11, 4, v11\n"
    "v_lshl_add_u32 v1, v8, 3, v11\n"
    "v_xor_b32 v2, 64, v1\n"
    "v_lshl_or_b32 v10, v9, 7, v10\n"
    "v_lshl_or_b32 v10, s46, 1, v10\n"
    "v_bfe_u32 v11, v8, 4, 1\n"
    "v_mul_u32_u24 v11, 0x28, v11\n"
    "v_xor_b32 v10, v10, v11\n"
    "s_mov_b32 s42, 30720\n"
    "v_lshl_add_u32 v3, v10, 3, s42\n"
    "v_add_u32 v5, 118784, v3\n"
    "v_mov_b32 v254, v3\n"
    "v_add_u32 v250, 0, v1\n"
    "v_add_u32 v251, 0, v2\n"
    "v_add_u32 v252, 65536, v1\n"
    "v_add_u32 v253, 65536, v2\n"
    "v_mov_b64 v[112:113], 0\n"
    "v_mov_b64 v[114:115], 0\n"
    "v_mov_b64 v[116:117], 0\n"
    "v_mov_b64 v[118:119], 0\n"
    "v_mov_b64 v[120:121], 0\n"
    "v_mov_b64 v[122:123], 0\n"
    "v_mov_b64 v[124:125], 0\n"
    "v_mov_b64 v[126:127], 0\n"
    "v_mov_b64 v[128:129], 0\n"
    "v_mov_b64 v[130:131], 0\n"
    "v_mov_b64 v[132:133], 0\n"
    "v_mov_b64 v[134:135], 0\n"
    "v_mov_b64 v[136:137], 0\n"
    "v_mov_b64 v[138:139], 0\n"
    "v_mov_b64 v[140:141], 0\n"
    "v_mov_b64 v[142:143], 0\n"
    "v_mov_b64 v[144:145], 0\n"
    "v_mov_b64 v[146:147], 0\n"
    "v_mov_b64 v[148:149], 0\n"
    "v_mov_b64 v[150:151], 0\n"
    "v_mov_b64 v[152:153], 0\n"
    "v_mov_b64 v[154:155], 0\n"
    "v_mov_b64 v[156:157], 0\n"
    "v_mov_b64 v[158:159], 0\n"
    "v_mov_b64 v[160:161], 0\n"
    "v_mov_b64 v[162:163], 0\n"
    "v_mov_b64 v[164:165], 0\n"
    "v_mov_b64 v[166:167], 0\n"
    "v_mov_b64 v[168:169], 0\n"
    "v_mov_b64 v[170:171], 0\n"
    "v_mov_b64 v[172:173], 0\n"
    "v_mov_b64 v[174:175], 0\n"
    "v_mov_b64 v[176:177], 0\n"
    "v_mov_b64 v[178:179], 0\n"
    "v_mov_b64 v[180:181], 0\n"
    "v_mov_b64 v[182:183], 0\n"
    "v_mov_b64 v[184:185], 0\n"
    "v_mov_b64 v[186:187], 0\n"
    "v_mov_b64 v[188:189], 0\n"
    "v_mov_b64 v[190:191], 0\n"
    "v_mov_b64 v[192:193], 0\n"
    "v_mov_b64 v[194:195], 0\n"
    "v_mov_b64 v[196:197], 0\n"
    "v_mov_b64 v[198:199], 0\n"
    "v_mov_b64 v[200:201], 0\n"
    "v_mov_b64 v[202:203], 0\n"
    "v_mov_b64 v[204:205], 0\n"
    "v_mov_b64 v[206:207], 0\n"
    "v_mov_b64 v[208:209], 0\n"
    "v_mov_b64 v[210:211], 0\n"
    "v_mov_b64 v[212:213], 0\n"
    "v_mov_b64 v[214:215], 0\n"
    "v_mov_b64 v[216:217], 0\n"
    "v_mov_b64 v[218:219], 0\n"
    "v_mov_b64 v[220:221], 0\n"
    "v_mov_b64 v[222:223], 0\n"
    "v_mov_b64 v[224:225], 0\n"
    "v_mov_b64 v[226:227], 0\n"
    "v_mov_b64 v[228:229], 0\n"
    "v_mov_b64 v[230:231], 0\n"
    "s_mov_b64 exec, 0\n"
    "s_cmp_eq_u32 s66, 0\n"
    "s_cbranch_scc1 151f\n"
    "s_cmp_eq_u32 s66, 1\n"
    "s_cbranch_scc1 152f\n"
    "s_cmp_eq_u32 s66, 2\n"
    "s_cbranch_scc1 141f\n"
    "s_branch 142f\n"
    "41:\n"
    "s_mov_b64 exec, -1\n"
    "s_waitcnt vmcnt(0)\n"
    "s_mov_b32 s42, 0x4b800000\n"
    "s_mov_b32 s43, 0xffff0000\n"
    "v_lshlrev_b32 v36, 16, v92\n"
    "v_lshlrev_b32 v37, 16, v96\n"
    "v_and_b32 v38, s43, v92\n"
    "v_and_b32 v39, s43, v96\n"
    "v_lshlrev_b32 v40, 16, v93\n"
    "v_lshlrev_b32 v41, 16, v97\n"
    "v_and_b32 v42, s43, v93\n"
    "v_and_b32 v43, s43, v97\n"
    "v_lshlrev_b32 v44, 16, v94\n"
    "v_lshlrev_b32 v45, 16, v98\n"
    "v_and_b32 v46, s43, v94\n"
    "v_and_b32 v47, s43, v98\n"
    "v_lshlrev_b32 v48, 16, v95\n"
    "v_lshlrev_b32 v49, 16, v99\n"
    "v_and_b32 v50, s43, v95\n"
    "v_and_b32 v51, s43, v99\n"
    "v_mul_f32 v37, -v37, v36\n"
    "v_mul_f32 v39, -v39, v38\n"
    "v_mul_f32 v41, -v41, v40\n"
    "v_mul_f32 v43, -v43, v42\n"
    "v_mul_f32 v45, -v45, v44\n"
    "v_mul_f32 v47, -v47, v46\n"
    "v_mul_f32 v49, -v49, v48\n"
    "v_mul_f32 v51, -v51, v50\n"
    "v_mul_f32 v36, s42, v36\n"
    "v_mul_f32 v38, s42, v38\n"
    "v_mul_f32 v40, s42, v40\n"
    "v_mul_f32 v42, s42, v42\n"
    "v_mul_f32 v44, s42, v44\n"
    "v_mul_f32 v46, s42, v46\n"
    "v_mul_f32 v48, s42, v48\n"
    "v_mul_f32 v50, s42, v50\n"
    "ds_write_b128 v16, v[36:39] offset:0\n"
    "ds_write_b128 v17, v[40:43] offset:256\n"
    "ds_write_b128 v18, v[44:47] offset:512\n"
    "ds_write_b128 v19, v[48:51] offset:768\n"
    "v_lshlrev_b32 v20, 16, v100\n"
    "v_lshlrev_b32 v21, 16, v104\n"
    "v_and_b32 v22, s43, v100\n"
    "v_and_b32 v23, s43, v104\n"
    "v_lshlrev_b32 v24, 16, v101\n"
    "v_lshlrev_b32 v25, 16, v105\n"
    "v_and_b32 v26, s43, v101\n"
    "v_and_b32 v27, s43, v105\n"
    "v_lshlrev_b32 v28, 16, v102\n"
    "v_lshlrev_b32 v29, 16, v106\n"
    "v_and_b32 v30, s43, v102\n"
    "v_and_b32 v31, s43, v106\n"
    "v_lshlrev_b32 v32, 16, v103\n"
    "v_lshlrev_b32 v33, 16, v107\n"
    "v_and_b32 v34, s43, v103\n"
    "v_and_b32 v35, s43, v107\n"
    "v_mul_f32 v21, -v21, v20\n"
    "v_mul_f32 v23, -v23, v22\n"
    "v_mul_f32 v25, -v25, v24\n"
    "v_mul_f32 v27, -v27, v26\n"
    "v_mul_f32 v29, -v29, v28\n"
    "v_mul_f32 v31, -v31, v30\n"
    "v_mul_f32 v33, -v33, v32\n"
    "v_mul_f32 v35, -v35, v34\n"
    "v_mul_f32 v20, s42, v20\n"
    "v_mul_f32 v22, s42, v22\n"
    "v_mul_f32 v24, s42, v24\n"
    "v_mul_f32 v26, s42, v26\n"
    "v_mul_f32 v28, s42, v28\n"
    "v_mul_f32 v30, s42, v30\n"
    "v_mul_f32 v32, s42, v32\n"
    "v_mul_f32 v34, s42, v34\n"
    "ds_write_b128 v16, v[20:23] offset:16384\n"
    "ds_write_b128 v17, v[24:27] offset:16640\n"
    "ds_write_b128 v18, v[28:31] offset:16896\n"
    "ds_write_b128 v19, v[32:35] offset:17152\n"
    "s_mov_b32 s53, 12\n"
    "s_mov_b32 s55, 38912\n"
    "s_mov_b32 s56, -77824\n"
    "s_mov_b32 s51, 0x000f000f\n"
    "s_mov_b32 s57, 0x492\n"
    "s_waitcnt lgkmcnt(0)\n"
    "s_barrier\n"
    "ds_read_b128 v[52:55], v1 offset:0\n"
    "ds_read_b128 v[56:59], v1 offset:2048\n"
    "ds_read_b128 v[60:63], v1 offset:4096\n"
    "ds_read_b128 v[64:67], v1 offset:6144\n"
    "ds_read_b128 v[68:71], v1 offset:8192\n"
    "ds_read_b128 v[72:75], v1 offset:10240\n"
    "ds_read_b128 v[76:79], v1 offset:12288\n"
    "ds_read_b128 v[80:83], v1 offset:14336\n"
    "ds_read_b128 v[84:87], v1 offset:16384\n"
    "ds_read_b128 v[88:91], v1 offset:18432\n"
    "ds_read_b128 v[92:95], v1 offset:20480\n"
    "ds_read_b128 v[96:99], v1 offset:22528\n"
    "ds_read_b128 v[100:103], v1 offset:24576\n"
    "ds_read_b128 v[104:107], v1 offset:26624\n"
    "s_cmp_lg_u32 s53, s53\n"
    "7:\n"
    "v_and_b32 v8, s51, v20\n"
    "v_pk_lshrrev_b16 v9, 12, v20 op_sel_hi:[0,1]\n"
    "v_fma_mix_f32 v10, v8, v28, v29 op_sel_hi:[1,0,0]\n"
    "v_fma_mix_f32 v11, v8, v28, v29 op_sel:[1,0,0] op_sel_hi:[1,0,0]\n"
    "v_fma_mix_f32 v12, v9, v30, v31 op_sel_hi:[1,0,0]\n"
    "v_fma_mix_f32 v13, v9, v30, v31 op_sel:[1,0,0] op_sel_hi:[1,0,0]\n"
    "v_cvt_pk_bf16_f32 v36, v10, v11\n"
    "v_cvt_pk_bf16_f32 v40, v12, v13\n"
    "v_and_b32 v14, s51, v21\n"
    "v_pk_lshrrev_b16 v15, 12, v21 op_sel_hi:[0,1]\n"
    "v_fma_mix_f32 v16, v14, v28, v29 op_sel_hi:[1,0,0]\n"
    "v_fma_mix_f32 v17, v14, v28, v29 op_sel:[1,0,0] op_sel_hi:[1,0,0]\n"
    "v_fma_mix_f32 v18, v15, v30, v31 op_sel_hi:[1,0,0]\n"
    "v_fma_mix_f32 v19, v15, v30, v31 op_sel:[1,0,0] op_sel_hi:[1,0,0]\n"
    "v_cvt_pk_bf16_f32 v37, v16, v17\n"
    "v_cvt_pk_bf16_f32 v41, v18, v19\n"
    "v_and_b32 v8, s51, v22\n"
    "v_pk_lshrrev_b16 v9, 12, v22 op_sel_hi:[0,1]\n"
    "v_fma_mix_f32 v10, v8, v28, v29 op_sel_hi:[1,0,0]\n"
    "v_fma_mix_f32 v11, v8, v28, v29 op_sel:[1,0,0] op_sel_hi:[1,0,0]\n"
    "v_fma_mix_f32 v12, v9, v30, v31 op_sel_hi:[1,0,0]\n"
    "v_fma_mix_f32 v13, v9, v30, v31 op_sel:[1,0,0] op_sel_hi:[1,0,0]\n"
    "v_cvt_pk_bf16_f32 v38, v10, v11\n"
    "v_cvt_pk_bf16_f32 v42, v12, v13\n"
    "v_and_b32 v14, s51, v23\n"
    "v_pk_lshrrev_b16 v15, 12, v23 op_sel_hi:[0,1]\n"
    "v_fma_mix_f32 v16, v14, v28, v29 op_sel_hi:[1,0,0]\n"
    "v_fma_mix_f32 v17, v14, v28, v29 op_sel:[1,0,0] op_sel_hi:[1,0,0]\n"
    "v_fma_mix_f32 v18, v15, v30, v31 op_sel_hi:[1,0,0]\n"
    "v_fma_mix_f32 v19, v15, v30, v31 op_sel:[1,0,0] op_sel_hi:[1,0,0]\n"
    "v_cvt_pk_bf16_f32 v39, v16, v17\n"
    "v_cvt_pk_bf16_f32 v43, v18, v19\n"
    "s_cbranch_scc1 8f\n"
    "ds_read_b128 v[28:31], v4\n"
    "ds_read_b128 v[32:35], v4 offset:1024\n"
    "s_cmp_eq_u32 s53, s53\n"
    "s_barrier\n"
    "ds_read2st64_b64 v[20:23], v5 offset0:0 offset1:1\n"
    "ds_read2st64_b64 v[24:27], v3 offset0:0 offset1:1\n"
    "s_waitcnt lgkmcnt(0)\n"
    "s_branch 7b\n"
    "8:\n"
    "s_cmp_eq_u32 s36, 14\n"
    "s_cbranch_scc1 14f\n"
    "s_branch 15f\n"
    ".p2align 3\n"
    "20:\n"
    "v_mfma_f32_16x16x32_bf16 v[112:115], v[44:47], v[52:55], v[112:115]\n"
    "ds_read_b128 v[108:111], v2 offset:28672\n"
    "v_and_b32 v8, s51, v20\n"
    "v_mfma_f32_16x16x32_bf16 v[116:119], v[48:51], v[52:55], v[116:119]\n"
    "v_pk_lshrrev_b16 v9, 12, v20 op_sel_hi:[0,1]\n"
    "v_fma_mix_f32 v10, v8, v28, v29 op_sel_hi:[1,0,0]\n"
    "v_mfma_f32_16x16x32_bf16 v[120:123], v[44:47], v[56:59], v[120:123]\n"
    "v_fma_mix_f32 v11, v8, v28, v29 op_sel:[1,0,0] op_sel_hi:[1,0,0]\n"
    "v_mfma_f32_16x16x32_bf16 v[124:127], v[48:51], v[56:59], v[124:127]\n"
    "v_fma_mix_f32 v12, v9, v30, v31 op_sel_hi:[1,0,0]\n"
    "v_mfma_f32_16x16x32_bf16 v[128:131], v[44:47], v[60:63], v[128:131]\n"
    "v_fma_mix_f32 v13, v9, v30, v31 op_sel:[1,0,0] op_sel_hi:[1,0,0]\n"
    "v_cvt_pk_bf16_f32 v36, v10, v11\n"
    "v_mfma_f32_16x16x32_bf16 v[132:135], v[48:51], v[60:63], v[132:135]\n"
    "s_waitcnt lgkmcnt(0)\n"
    "v_cvt_pk_bf16_f32 v40, v12, v13\n"
    "v_mfma_f32_16x16x32_bf16 v[136:139], v[44:47], v[64:67], v[136:139]\n"
    "s_barrier\n"
    "ds_read_b128 v[52:55], v1 offset:0\n"
    "v_and_b32 v14, s51, v21\n"
    "v_mfma_f32_16x16x32_bf16 v[140:143], v[48:51], v[64:67], v[140:143]\n"
    "ds_read2st64_b64 v[24:27], v3 offset0:0 offset1:1\n"
    "v_pk_lshrrev_b16 v15, 12, v21 op_sel_hi:[0,1]\n"
    "v_mfma_f32_16x16x32_bf16 v[144:147], v[44:47], v[68:71], v[144:147]\n"
    "ds_read_b128 v[56:59], v1 offset:2048\n"
    "v_fma_mix_f32 v16, v14, v28, v29 op_sel_hi:[1,0,0]\n"
    "v_mfma_f32_16x16x32_bf16 v[148:151], v[48:51], v[68:71], v[148:151]\n"
    "ds_read_b128 v[32:35], v4 offset:3072\n"
    "v_fma_mix_f32 v17, v14, v28, v29 op_sel:[1,0,0] op_sel_hi:[1,0,0]\n"
    "v_mfma_f32_16x16x32_bf16 v[152:155], v[44:47], v[72:75], v[152:155]\n"
    "ds_read_b128 v[60:63], v1 offset:4096\n"
    "v_fma_mix_f32 v18, v15, v30, v31 op_sel_hi:[1,0,0]\n"
    "v_mfma_f32_16x16x32_bf16 v[156:159], v[48:51], v[72:75], v[156:159]\n"
    "ds_read_b128 v[64:67], v1 offset:6144\n"
    "v_fma_mix_f32 v19, v15, v30, v31 op_sel:[1,0,0] op_sel_hi:[1,0,0]\n"
    "v_mfma_f32_16x16x32_bf16 v[160:163], v[44:47], v[76:79], v[160:163]\n"
    "ds_read_b128 v[68:71], v1 offset:8192\n"
    "v_cvt_pk_bf16_f32 v37, v16, v17\n"
    "v_mfma_f32_16x16x32_bf16 v[164:167], v[48:51], v[76:79], v[164:167]\n"
    "ds_read_b128 v[72:75], v1 offset:10240\n"
    "v_cvt_pk_bf16_f32 v41, v18, v19\n"
    "v_mfma_f32_16x16x32_bf16 v[168:171], v[44:47], v[80:83], v[168:171]\n"
    "ds_read_b128 v[76:79], v1 offset:12288\n"
    "v_and_b32 v8, s51, v22\n"
    "v_mfma_f32_16x16x32_bf16 v[172:175], v[48:51], v[80:83], v[172:175]\n"
    "v_pk_lshrrev_b16 v9, 12, v22 op_sel_hi:[0,1]\n"
    "v_mfma_f32_16x16x32_bf16 v[176:179], v[44:47], v[84:87], v[176:179]\n"
    "ds_read_b128 v[80:83], v1 offset:14336\n"
    "v_fma_mix_f32 v10, v8, v28, v29 op_sel_hi:[1,0,0]\n"
    "v_mfma_f32_16x16x32_bf16 v[180:183], v[48:51], v[84:87], v[180:183]\n"
    "v_fma_mix_f32 v11, v8, v28, v29 op_sel:[1,0,0] op_sel_hi:[1,0,0]\n"
    "v_fma_mix_f32 v12, v9, v30, v31 op_sel_hi:[1,0,0]\n"
    "v_mfma_f32_16x16x32_bf16 v[184:187], v[44:47], v[88:91], v[184:187]\n"
    "ds_read_b128 v[84:87], v1 offset:16384\n"
    "v_fma_mix_f32 v13, v9, v30, v31 op_sel:[1,0,0] op_sel_hi:[1,0,0]\n"
    "v_mfma_f32_16x16x32_bf16 v[188:191], v[48:51], v[88:91], v[188:191]\n"
    "v_cvt_pk_bf16_f32 v38, v10, v11\n"
    "v_mfma_f32_16x16x32_bf16 v[192:195], v[44:47], v[92:95], v[192:195]\n"
    "ds_read_b128 v[88:91], v1 offset:18432\n"
    "v_cvt_pk_bf16_f32 v42, v12, v13\n"
    "v_mfma_f32_16x16x32_bf16 v[196:199], v[48:51], v[92:95], v[196:199]\n"
    "v_and_b32 v14, s51, v23\n"
    "v_mfma_f32_16x16x32_bf16 v[200:203], v[44:47], v[96:99], v[200:203]\n"
    "ds_read_b128 v[92:95], v1 offset:20480\n"
    "v_pk_lshrrev_b16 v15, 12, v23 op_sel_hi:[0,1]\n"
    "v_mfma_f32_16x16x32_bf16 v[204:207], v[48:51], v[96:99], v[204:207]\n"
    "v_fma_mix_f32 v16, v14, v28, v29 op_sel_hi:[1,0,0]\n"
    "v_fma_mix_f32 v17, v14, v28, v29 op_sel:[1,0,0] op_sel_hi:[1,0,0]\n"
    "v_mfma_f32_16x16x32_bf16 v[208:211], v[44:47], v[100:103], v[208:211]\n"
    "ds_read_b128 v[96:99], v1 offset:22528\n"
    "v_fma_mix_f32 v18, v15, v30, v31 op_sel_hi:[1,0,0]\n"
    "v_mfma_f32_16x16x32_bf16 v[212:215], v[48:51], v[100:103], v[212:215]\n"
    "v_fma_mix_f32 v19, v15, v30, v31 op_sel:[1,0,0] op_sel_hi:[1,0,0]\n"
    "v_mfma_f32_16x16x32_bf16 v[216:219], v[44:47], v[104:107], v[216:219]\n"
    "ds_read_b128 v[100:103], v1 offset:24576\n"
    "v_cvt_pk_bf16_f32 v39, v16, v17\n"
    "v_mfma_f32_16x16x32_bf16 v[220:223], v[48:51], v[104:107], v[220:223]\n"
    "v_cvt_pk_bf16_f32 v43, v18, v19\n"
    "v_mfma_f32_16x16x32_bf16 v[224:227], v[44:47], v[108:111], v[224:227]\n"
    "s_waitcnt lgkmcnt(3)\n"
    "ds_read_b128 v[104:107], v1 offset:26624\n"
    "v_add_u32 v2, s54, v2\n"
    "v_mfma_f32_16x16x32_bf16 v[228:231], v[48:51], v[108:111], v[228:231]\n"
    "v_add_u32 v4, 2048, v4\n"
    "15:\n"
    "v_mfma_f32_16x16x32_bf16 v[112:115], v[36:39], v[52:55], v[112:115]\n"
    "s_bitcmp1_b32 s57, s53\n"
    "ds_read_b128 v[108:111], v1 offset:28672\n"
    "v_and_b32 v8, s51, v24\n"
    "v_mfma_f32_16x16x32_bf16 v[116:119], v[40:43], v[52:55], v[116:119]\n"
    "s_cselect_b32 s54, s56, s55\n"
    "ds_read2st64_b64 v[20:23], v3 offset0:8 offset1:9\n"
    "v_pk_lshrrev_b16 v9, 12, v24 op_sel_hi:[0,1]\n"
    "v_mfma_f32_16x16x32_bf16 v[120:123], v[36:39], v[56:59], v[120:123]\n"
    "s_sub_u32 s53, s53, 1\n"
    "ds_read_b128 v[52:55], v2 offset:0\n"
    "v_fma_mix_f32 v10, v8, v32, v33 op_sel_hi:[1,0,0]\n"
    "v_mfma_f32_16x16x32_bf16 v[124:127], v[40:43], v[56:59], v[124:127]\n"
    "ds_read_b128 v[28:31], v4 offset:2048\n"
    "v_fma_mix_f32 v11, v8, v32, v33 op_sel:[1,0,0] op_sel_hi:[1,0,0]\n"
    "v_mfma_f32_16x16x32_bf16 v[128:131], v[36:39], v[60:63], v[128:131]\n"
    "ds_read_b128 v[56:59], v2 offset:2048\n"
    "v_fma_mix_f32 v12, v9, v34, v35 op_sel_hi:[1,0,0]\n"
    "v_mfma_f32_16x16x32_bf16 v[132:135], v[40:43], v[60:63], v[132:135]\n"
    "v_fma_mix_f32 v13, v9, v34, v35 op_sel:[1,0,0] op_sel_hi:[1,0,0]\n"
    "v_cvt_pk_bf16_f32 v44, v10, v11\n"
    "v_mfma_f32_16x16x32_bf16 v[136:139], v[36:39], v[64:67], v[136:139]\n"
    "ds_read_b128 v[60:63], v2 offset:4096\n"
    "v_cvt_pk_bf16_f32 v48, v12, v13\n"
    "v_mfma_f32_16x16x32_bf16 v[140:143], v[40:43], v[64:67], v[140:143]\n"
    "v_and_b32 v14, s51, v25\n"
    "v_mfma_f32_16x16x32_bf16 v[144:147], v[36:39], v[68:71], v[144:147]\n"
    "ds_read_b128 v[64:67], v2 offset:6144\n"
    "v_pk_lshrrev_b16 v15, 12, v25 op_sel_hi:[0,1]\n"
    "v_mfma_f32_16x16x32_bf16 v[148:151], v[40:43], v[68:71], v[148:151]\n"
    "v_fma_mix_f32 v16, v14, v32, v33 op_sel_hi:[1,0,0]\n"
    "v_fma_mix_f32 v17, v14, v32, v33 op_sel:[1,0,0] op_sel_hi:[1,0,0]\n"
    "v_mfma_f32_16x16x32_bf16 v[152:155], v[36:39], v[72:75], v[152:155]\n"
    "ds_read_b128 v[68:71], v2 offset:8192\n"
    "v_fma_mix_f32 v18, v15, v34, v35 op_sel_hi:[1,0,0]\n"
    "v_mfma_f32_16x16x32_bf16 v[156:159], v[40:43], v[72:75], v[156:159]\n"
    "v_fma_mix_f32 v19, v15, v34, v35 op_sel:[1,0,0] op_sel_hi:[1,0,0]\n"
    "v_mfma_f32_16x16x32_bf16 v[160:163], v[36:39], v[76:79], v[160:163]\n"
    "ds_read_b128 v[72:75], v2 offset:10240\n"
    "v_cvt_pk_bf16_f32 v45, v16, v17\n"
    "v_mfma_f32_16x16x32_bf16 v[164:167], v[40:43], v[76:79], v[164:167]\n"
    "v_cvt_pk_bf16_f32 v49, v18, v19\n"
    "v_and_b32 v8, s51, v26\n"
    "v_mfma_f32_16x16x32_bf16 v[168:171], v[36:39], v[80:83], v[168:171]\n"
    "ds_read_b128 v[76:79], v2 offset:12288\n"
    "v_pk_lshrrev_b16 v9, 12, v26 op_sel_hi:[0,1]\n"
    "v_mfma_f32_16x16x32_bf16 v[172:175], v[40:43], v[80:83], v[172:175]\n"
    "v_fma_mix_f32 v10, v8, v32, v33 op_sel_hi:[1,0,0]\n"
    "v_mfma_f32_16x16x32_bf16 v[176:179], v[36:39], v[84:87], v[176:179]\n"
    "ds_read_b128 v[80:83], v2 offset:14336\n"
    "v_fma_mix_f32 v11, v8, v32, v33 op_sel:[1,0,0] op_sel_hi:[1,0,0]\n"
    "v_mfma_f32_16x16x32_bf16 v[180:183], v[40:43], v[84:87], v[180:183]\n"
    "v_fma_mix_f32 v12, v9, v34, v35 op_sel_hi:[1,0,0]\n"
    "v_mfma_f32_16x16x32_bf16 v[184:187], v[36:39], v[88:91], v[184:187]\n"
    "ds_read_b128 v[84:87], v2 offset:16384\n"
    "v_fma_mix_f32 v13, v9, v34, v35 op_sel:[1,0,0] op_sel_hi:[1,0,0]\n"
    "v_mfma_f32_16x16x32_bf16 v[188:191], v[40:43], v[88:91], v[188:191]\n"
    "s_waitcnt lgkmcnt(4)\n"
    "v_cvt_pk_bf16_f32 v46, v10, v11\n"
    "v_cvt_pk_bf16_f32 v50, v12, v13\n"
    "v_mfma_f32_16x16x32_bf16 v[192:195], v[36:39], v[92:95], v[192:195]\n"
    "ds_read_b128 v[88:91], v2 offset:18432\n"
    "v_and_b32 v14, s51, v27\n"
    "v_mfma_f32_16x16x32_bf16 v[196:199], v[40:43], v[92:95], v[196:199]\n"
    "v_pk_lshrrev_b16 v15, 12, v27 op_sel_hi:[0,1]\n"
    "v_mfma_f32_16x16x32_bf16 v[200:203], v[36:39], v[96:99], v[200:203]\n"
    "ds_read_b128 v[92:95], v2 offset:20480\n"
    "v_fma_mix_f32 v16, v14, v32, v33 op_sel_hi:[1,0,0]\n"
    "v_mfma_f32_16x16x32_bf16 v[204:207], v[40:43], v[96:99], v[204:207]\n"
    "v_fma_mix_f32 v17, v14, v32, v33 op_sel:[1,0,0] op_sel_hi:[1,0,0]\n"
    "v_fma_mix_f32 v18, v15, v34, v35 op_sel_hi:[1,0,0]\n"
    "v_mfma_f32_16x16x32_bf16 v[208:211], v[36:39], v[100:103], v[208:211]\n"
    "ds_read_b128 v[96:99], v2 offset:22528\n"
    "v_fma_mix_f32 v19, v15, v34, v35 op_sel:[1,0,0] op_sel_hi:[1,0,0]\n"
    "v_mfma_f32_16x16x32_bf16 v[212:215], v[40:43], v[100:103], v[212:215]\n"
    "v_cvt_pk_bf16_f32 v47, v16, v17\n"
    "v_mfma_f32_16x16x32_bf16 v[216:219], v[36:39], v[104:107], v[216:219]\n"
    "ds_read_b128 v[100:103], v2 offset:24576\n"
    "v_cvt_pk_bf16_f32 v51, v18, v19\n"
    "v_mfma_f32_16x16x32_bf16 v[220:223], v[40:43], v[104:107], v[220:223]\n"
    "v_add_u32 v1, s54, v1\n"
    "v_mfma_f32_16x16x32_bf16 v[224:227], v[36:39], v[108:111], v[224:227]\n"
    "ds_read_b128 v[104:107], v2 offset:26624\n"
    "v_add_u32 v3, s54, v3\n"
    "v_mfma_f32_16x16x32_bf16 v[228:231], v[40:43], v[108:111], v[228:231]\n"
    "s_cbranch_scc0 20b\n"
    "31:\n"
    "v_mfma_f32_16x16x32_bf16 v[112:115], v[44:47], v[52:55], v[112:115]\n"
    "ds_read_b128 v[108:111], v2 offset:28672\n"
    "v_and_b32 v8, s51, v20\n"
    "v_mfma_f32_16x16x32_bf16 v[116:119], v[48:51], v[52:55], v[116:119]\n"
    "v_pk_lshrrev_b16 v9, 12, v20 op_sel_hi:[0,1]\n"
    "v_fma_mix_f32 v10, v8, v28, v29 op_sel_hi:[1,0,0]\n"
    "v_mfma_f32_16x16x32_bf16 v[120:123], v[44:47], v[56:59], v[120:123]\n"
    "v_fma_mix_f32 v11, v8, v28, v29 op_sel:[1,0,0] op_sel_hi:[1,0,0]\n"
    "v_fma_mix_f32 v12, v9, v30, v31 op_sel_hi:[1,0,0]\n"
    "v_mfma_f32_16x16x32_bf16 v[124:127], v[48:51], v[56:59], v[124:127]\n"
    "v_fma_mix_f32 v13, v9, v30, v31 op_sel:[1,0,0] op_sel_hi:[1,0,0]\n"
    "v_cvt_pk_bf16_f32 v36, v10, v11\n"
    "v_mfma_f32_16x16x32_bf16 v[128:131], v[44:47], v[60:63], v[128:131]\n"
    "v_cvt_pk_bf16_f32 v40, v12, v13\n"
    "v_and_b32 v14, s51, v21\n"
    "v_mfma_f32_16x16x32_bf16 v[132:135], v[48:51], v[60:63], v[132:135]\n"
    "s_waitcnt lgkmcnt(0)\n"
    "v_pk_lshrrev_b16 v15, 12, v21 op_sel_hi:[0,1]\n"
    "v_fma_mix_f32 v16, v14, v28, v29 op_sel_hi:[1,0,0]\n"
    "v_mfma_f32_16x16x32_bf16 v[136:139], v[44:47], v[64:67], v[136:139]\n"
    "s_barrier\n"
    "v_fma_mix_f32 v17, v14, v28, v29 op_sel:[1,0,0] op_sel_hi:[1,0,0]\n"
    "v_fma_mix_f32 v18, v15, v30, v31 op_sel_hi:[1,0,0]\n"
    "v_mfma_f32_16x16x32_bf16 v[140:143], v[48:51], v[64:67], v[140:143]\n"
    "v_fma_mix_f32 v19, v15, v30, v31 op_sel:[1,0,0] op_sel_hi:[1,0,0]\n"
    "v_cvt_pk_bf16_f32 v37, v16, v17\n"
    "v_mfma_f32_16x16x32_bf16 v[144:147], v[44:47], v[68:71], v[144:147]\n"
    "v_cvt_pk_bf16_f32 v41, v18, v19\n"
    "v_and_b32 v8, s51, v22\n"
    "v_mfma_f32_16x16x32_bf16 v[148:151], v[48:51], v[68:71], v[148:151]\n"
    "ds_read_b128 v[32:35], v4 offset:3072\n"
    "v_pk_lshrrev_b16 v9, 12, v22 op_sel_hi:[0,1]\n"
    "v_mfma_f32_16x16x32_bf16 v[152:155], v[44:47], v[72:75], v[152:155]\n"
    "ds_read2st64_b64 v[24:27], v254 offset0:76 offset1:77\n"
    "v_fma_mix_f32 v10, v8, v28, v29 op_sel_hi:[1,0,0]\n"
    "v_mfma_f32_16x16x32_bf16 v[156:159], v[48:51], v[72:75], v[156:159]\n"
    "v_fma_mix_f32 v11, v8, v28, v29 op_sel:[1,0,0] op_sel_hi:[1,0,0]\n"
    "v_fma_mix_f32 v12, v9, v30, v31 op_sel_hi:[1,0,0]\n"
    "v_mfma_f32_16x16x32_bf16 v[160:163], v[44:47], v[76:79], v[160:163]\n"
    "v_fma_mix_f32 v13, v9, v30, v31 op_sel:[1,0,0] op_sel_hi:[1,0,0]\n"
    "v_cvt_pk_bf16_f32 v38, v10, v11\n"
    "v_mfma_f32_16x16x32_bf16 v[164:167], v[48:51], v[76:79], v[164:167]\n"
    "v_cvt_pk_bf16_f32 v42, v12, v13\n"
    "v_and_b32 v14, s51, v23\n"
    "v_mfma_f32_16x16x32_bf16 v[168:171], v[44:47], v[80:83], v[168:171]\n"
    "v_pk_lshrrev_b16 v15, 12, v23 op_sel_hi:[0,1]\n"
    "v_fma_mix_f32 v16, v14, v28, v29 op_sel_hi:[1,0,0]\n"
    "v_mfma_f32_16x16x32_bf16 v[172:175], v[48:51], v[80:83], v[172:175]\n"
    "v_fma_mix_f32 v17, v14, v28, v29 op_sel:[1,0,0] op_sel_hi:[1,0,0]\n"
    "v_fma_mix_f32 v18, v15, v30, v31 op_sel_hi:[1,0,0]\n"
    "v_mfma_f32_16x16x32_bf16 v[176:179], v[44:47], v[84:87], v[176:179]\n"
    "v_fma_mix_f32 v19, v15, v30, v31 op_sel:[1,0,0] op_sel_hi:[1,0,0]\n"
    "v_cvt_pk_bf16_f32 v39, v16, v17\n"
    "v_mfma_f32_16x16x32_bf16 v[180:183], v[48:51], v[84:87], v[180:183]\n"
    "v_cvt_pk_bf16_f32 v43, v18, v19\n"
    "v_mfma_f32_16x16x32_bf16 v[184:187], v[44:47], v[88:91], v[184:187]\n"
    "v_mfma_f32_16x16x32_bf16 v[188:191], v[48:51], v[88:91], v[188:191]\n"
    "s_waitcnt lgkmcnt(0)\n"
    "v_and_b32 v8, s51, v24\n"
    "v_pk_lshrrev_b16 v9, 12, v24 op_sel_hi:[0,1]\n"
    "v_mfma_f32_16x16x32_bf16 v[192:195], v[44:47], v[92:95], v[192:195]\n"
    "ds_read_b128 v[52:55], v250 offset:38912\n"
    "v_fma_mix_f32 v10, v8, v32, v33 op_sel_hi:[1,0,0]\n"
    "v_fma_mix_f32 v11, v8, v32, v33 op_sel:[1,0,0] op_sel_hi:[1,0,0]\n"
    "v_mfma_f32_16x16x32_bf16 v[196:199], v[48:51], v[92:95], v[196:199]\n"
    "v_fma_mix_f32 v12, v9, v34, v35 op_sel_hi:[1,0,0]\n"
    "v_fma_mix_f32 v13, v9, v34, v35 op_sel:[1,0,0] op_sel_hi:[1,0,0]\n"
    "v_mfma_f32_16x16x32_bf16 v[200:203], v[44:47], v[96:99], v[200:203]\n"
    "ds_read_b128 v[56:59], v250 offset:40960\n"
    "v_cvt_pk_bf16_f32 v234, v10, v11\n"
    "v_cvt_pk_bf16_f32 v238, v12, v13\n"
    "v_mfma_f32_16x16x32_bf16 v[204:207], v[48:51], v[96:99], v[204:207]\n"
    "v_and_b32 v14, s51, v25\n"
    "v_pk_lshrrev_b16 v15, 12, v25 op_sel_hi:[0,1]\n"
    "v_mfma_f32_16x16x32_bf16 v[208:211], v[44:47], v[100:103], v[208:211]\n"
    "ds_read_b128 v[60:63], v250 offset:43008\n"
    "v_fma_mix_f32 v16, v14, v32, v33 op_sel_hi:[1,0,0]\n"
    "v_mfma_f32_16x16x32_bf16 v[212:215], v[48:51], v[100:103], v[212:215]\n"
    "v_fma_mix_f32 v17, v14, v32, v33 op_sel:[1,0,0] op_sel_hi:[1,0,0]\n"
    "v_fma_mix_f32 v18, v15, v34, v35 op_sel_hi:[1,0,0]\n"
    "v_mfma_f32_16x16x32_bf16 v[216:219], v[44:47], v[104:107], v[216:219]\n"
    "ds_read_b128 v[64:67], v250 offset:45056\n"
    "v_fma_mix_f32 v19, v15, v34, v35 op_sel:[1,0,0] op_sel_hi:[1,0,0]\n"
    "v_cvt_pk_bf16_f32 v235, v16, v17\n"
    "v_mfma_f32_16x16x32_bf16 v[220:223], v[48:51], v[104:107], v[220:223]\n"
    "ds_read_b128 v[28:31], v4 offset:4096\n"
    "v_cvt_pk_bf16_f32 v239, v18, v19\n"
    "v_and_b32 v8, s51, v26\n"
    "v_mfma_f32_16x16x32_bf16 v[224:227], v[44:47], v[108:111], v[224:227]\n"
    "ds_read2st64_b64 v[20:23], v254 offset0:84 offset1:85\n"
    "v_pk_lshrrev_b16 v9, 12, v26 op_sel_hi:[0,1]\n"
    "v_mfma_f32_16x16x32_bf16 v[228:231], v[48:51], v[108:111], v[228:231]\n"
    "s_waitcnt lgkmcnt(5)\n"
    "ds_read_b128 v[68:71], v251 offset:38912\n"
    "v_fma_mix_f32 v10, v8, v32, v33 op_sel_hi:[1,0,0]\n"
    "v_fma_mix_f32 v11, v8, v32, v33 op_sel:[1,0,0] op_sel_hi:[1,0,0]\n"
    "v_mfma_f32_16x16x32_bf16 v[112:115], v[36:39], v[52:55], v[112:115]\n"
    "ds_read_b128 v[72:75], v251 offset:40960\n"
    "v_fma_mix_f32 v12, v9, v34, v35 op_sel_hi:[1,0,0]\n"
    "v_fma_mix_f32 v13, v9, v34, v35 op_sel:[1,0,0] op_sel_hi:[1,0,0]\n"
    "v_mfma_f32_16x16x32_bf16 v[116:119], v[40:43], v[52:55], v[116:119]\n"
    "s_waitcnt lgkmcnt(6)\n"
    "v_cvt_pk_bf16_f32 v236, v10, v11\n"
    "v_cvt_pk_bf16_f32 v240, v12, v13\n"
    "v_mfma_f32_16x16x32_bf16 v[120:123], v[36:39], v[56:59], v[120:123]\n"
    "ds_read_b128 v[76:79], v251 offset:43008\n"
    "v_and_b32 v14, s51, v27\n"
    "v_mfma_f32_16x16x32_bf16 v[124:127], v[40:43], v[56:59], v[124:127]\n"
    "s_waitcnt lgkmcnt(6)\n"
    "v_pk_lshrrev_b16 v15, 12, v27 op_sel_hi:[0,1]\n"
    "v_fma_mix_f32 v16, v14, v32, v33 op_sel_hi:[1,0,0]\n"
    "v_mfma_f32_16x16x32_bf16 v[128:131], v[36:39], v[60:63], v[128:131]\n"
    "ds_read_b128 v[80:83], v251 offset:45056\n"
    "v_fma_mix_f32 v17, v14, v32, v33 op_sel:[1,0,0] op_sel_hi:[1,0,0]\n"
    "v_fma_mix_f32 v18, v15, v34, v35 op_sel_hi:[1,0,0]\n"
    "v_mfma_f32_16x16x32_bf16 v[132:135], v[40:43], v[60:63], v[132:135]\n"
    "s_waitcnt lgkmcnt(5)\n"
    "v_fma_mix_f32 v19, v15, v34, v35 op_sel:[1,0,0] op_sel_hi:[1,0,0]\n"
    "v_cvt_pk_bf16_f32 v237, v16, v17\n"
    "v_mfma_f32_16x16x32_bf16 v[136:139], v[36:39], v[64:67], v[136:139]\n"
    "ds_read_b128 v[84:87], v250 offset:47104\n"
    "v_cvt_pk_bf16_f32 v241, v18, v19\n"
    "v_mfma_f32_16x16x32_bf16 v[140:143], v[40:43], v[64:67], v[140:143]\n"
    "s_waitcnt lgkmcnt(4)\n"
    "v_and_b32 v8, s51, v20\n"
    "v_pk_lshrrev_b16 v9, 12, v20 op_sel_hi:[0,1]\n"
    "v_mfma_f32_16x16x32_bf16 v[112:115], v[234:237], v[68:71], v[112:115]\n"
    "ds_read_b128 v[88:91], v251 offset:47104\n"
    "v_fma_mix_f32 v10, v8, v28, v29 op_sel_hi:[1,0,0]\n"
    "v_fma_mix_f32 v11, v8, v28, v29 op_sel:[1,0,0] op_sel_hi:[1,0,0]\n"
    "v_mfma_f32_16x16x32_bf16 v[116:119], v[238:241], v[68:71], v[116:119]\n"
    "s_waitcnt lgkmcnt(4)\n"
    "v_fma_mix_f32 v12, v9, v30, v31 op_sel_hi:[1,0,0]\n"
    "v_fma_mix_f32 v13, v9, v30, v31 op_sel:[1,0,0] op_sel_hi:[1,0,0]\n"
    "v_mfma_f32_16x16x32_bf16 v[120:123], v[234:237], v[72:75], v[120:123]\n"
    "ds_read_b128 v[92:95], v250 offset:49152\n"
    "v_cvt_pk_bf16_f32 v44, v10, v11\n"
    "v_mfma_f32_16x16x32_bf16 v[124:127], v[238:241], v[72:75], v[124:127]\n"
    "s_waitcnt lgkmcnt(4)\n"
    "v_cvt_pk_bf16_f32 v48, v12, v13\n"
    "v_and_b32 v14, s51, v21\n"
    "v_mfma_f32_16x16x32_bf16 v[128:131], v[234:237], v[76:79], v[128:131]\n"
    "ds_read_b128 v[52:55], v251 offset:49152\n"
    "v_pk_lshrrev_b16 v15, 12, v21 op_sel_hi:[0,1]\n"
    "v_fma_mix_f32 v16, v14, v28, v29 op_sel_hi:[1,0,0]\n"
    "v_mfma_f32_16x16x32_bf16 v[132:135], v[238:241], v[76:79], v[132:135]\n"
    "s_waitcnt lgkmcnt(4)\n"
    "v_fma_mix_f32 v17, v14, v28, v29 op_sel:[1,0,0] op_sel_hi:[1,0,0]\n"
    "v_fma_mix_f32 v18, v15, v30, v31 op_sel_hi:[1,0,0]\n"
    "v_mfma_f32_16x16x32_bf16 v[136:139], v[234:237], v[80:83], v[136:139]\n"
    "ds_read_b128 v[56:59], v250 offset:51200\n"
    "v_fma_mix_f32 v19, v15, v30, v31 op_sel:[1,0,0] op_sel_hi:[1,0,0]\n"
    "v_mfma_f32_16x16x32_bf16 v[140:143], v[238:241], v[80:83], v[140:143]\n"
    "s_waitcnt lgkmcnt(4)\n"
    "v_cvt_pk_bf16_f32 v45, v16, v17\n"
    "v_cvt_pk_bf16_f32 v49, v18, v19\n"
    "v_mfma_f32_16x16x32_bf16 v[144:147], v[36:39], v[84:87], v[144:147]\n"
    "ds_read_b128 v[32:35], v4 offset:5120\n"
    "v_and_b32 v8, s51, v22\n"
    "v_pk_lshrrev_b16 v9, 12, v22 op_sel_hi:[0,1]\n"
    "v_mfma_f32_16x16x32_bf16 v[148:151], v[40:43], v[84:87], v[148:151]\n"
    "s_waitcnt lgkmcnt(4)\n"
    "ds_read_b128 v[60:63], v251 offset:51200\n"
    "v_fma_mix_f32 v10, v8, v28, v29 op_sel_hi:[1,0,0]\n"
    "v_mfma_f32_16x16x32_bf16 v[144:147], v[234:237], v[88:91], v[144:147]\n"
    "v_fma_mix_f32 v11, v8, v28, v29 op_sel:[1,0,0] op_sel_hi:[1,0,0]\n"
    "v_fma_mix_f32 v12, v9, v30, v31 op_sel_hi:[1,0,0]\n"
    "v_mfma_f32_16x16x32_bf16 v[148:151], v[238:241], v[88:91], v[148:151]\n"
    "s_waitcnt lgkmcnt(4)\n"
    "v_fma_mix_f32 v13, v9, v30, v31 op_sel:[1,0,0] op_sel_hi:[1,0,0]\n"
    "v_cvt_pk_bf16_f32 v46, v10, v11\n"
    "v_mfma_f32_16x16x32_bf16 v[152:155], v[36:39], v[92:95], v[152:155]\n"
    "s_barrier\n"
    "ds_read2st64_b64 v[24:27], v254 offset0:152 offset1:153\n"
    "v_cvt_pk_bf16_f32 v50, v12, v13\n"
    "v_and_b32 v14, s51, v23\n"
    "v_mfma_f32_16x16x32_bf16 v[156:159], v[40:43], v[92:95], v[156:159]\n"
    "s_waitcnt lgkmcnt(4)\n"
    "ds_read_b128 v[64:67], v252 offset:12288\n"
    "v_pk_lshrrev_b16 v15, 12, v23 op_sel_hi:[0,1]\n"
    "v_mfma_f32_16x16x32_bf16 v[152:155], v[234:237], v[52:55], v[152:155]\n"
    "ds_read_b128 v[68:71], v252 offset:14336\n"
    "v_fma_mix_f32 v16, v14, v28, v29 op_sel_hi:[1,0,0]\n"
    "v_fma_mix_f32 v17, v14, v28, v29 op_sel:[1,0,0] op_sel_hi:[1,0,0]\n"
    "v_mfma_f32_16x16x32_bf16 v[156:159], v[238:241], v[52:55], v[156:159]\n"
    "s_waitcnt lgkmcnt(5)\n"
    "ds_read_b128 v[72:75], v252 offset:16384\n"
    "v_fma_mix_f32 v18, v15, v30, v31 op_sel_hi:[1,0,0]\n"
    "v_mfma_f32_16x16x32_bf16 v[160:163], v[36:39], v[56:59], v[160:163]\n"
    "ds_read_b128 v[76:79], v252 offset:18432\n"
    "v_fma_mix_f32 v19, v15, v30, v31 op_sel:[1,0,0] op_sel_hi:[1,0,0]\n"
    "v_mfma_f32_16x16x32_bf16 v[164:167], v[40:43], v[56:59], v[164:167]\n"
    "s_waitcnt lgkmcnt(5)\n"
    "v_cvt_pk_bf16_f32 v47, v16, v17\n"
    "v_cvt_pk_bf16_f32 v51, v18, v19\n"
    "v_mfma_f32_16x16x32_bf16 v[160:163], v[234:237], v[60:63], v[160:163]\n"
    "s_waitcnt lgkmcnt(4)\n"
    "ds_read_b128 v[80:83], v252 offset:20480\n"
    "v_and_b32 v8, s51, v24\n"
    "v_pk_lshrrev_b16 v9, 12, v24 op_sel_hi:[0,1]\n"
    "v_mfma_f32_16x16x32_bf16 v[164:167], v[238:241], v[60:63], v[164:167]\n"
    "s_waitcnt lgkmcnt(4)\n"
    "v_fma_mix_f32 v10, v8, v32, v33 op_sel_hi:[1,0,0]\n"
    "v_fma_mix_f32 v11, v8, v32, v33 op_sel:[1,0,0] op_sel_hi:[1,0,0]\n"
    "v_mfma_f32_16x16x32_bf16 v[112:115], v[44:47], v[64:67], v[112:115]\n"
    "ds_read_b128 v[84:87], v252 offset:22528\n"
    "v_fma_mix_f32 v12, v9, v34, v35 op_sel_hi:[1,0,0]\n"
    "v_fma_mix_f32 v13, v9, v34, v35 op_sel:[1,0,0] op_sel_hi:[1,0,0]\n"
    "v_mfma_f32_16x16x32_bf16 v[116:119], v[48:51], v[64:67], v[116:119]\n"
    "s_waitcnt lgkmcnt(4)\n"
    "v_cvt_pk_bf16_f32 v242, v10, v11\n"
    "v_cvt_pk_bf16_f32 v246, v12, v13\n"
    "v_mfma_f32_16x16x32_bf16 v[120:123], v[44:47], v[68:71], v[120:123]\n"
    "ds_read_b128 v[88:91], v252 offset:24576\n"
    "v_and_b32 v14, s51, v25\n"
    "v_mfma_f32_16x16x32_bf16 v[124:127], v[48:51], v[68:71], v[124:127]\n"
    "s_waitcnt lgkmcnt(4)\n"
    "v_pk_lshrrev_b16 v15, 12, v25 op_sel_hi:[0,1]\n"
    "v_fma_mix_f32 v16, v14, v32, v33 op_sel_hi:[1,0,0]\n"
    "v_mfma_f32_16x16x32_bf16 v[128:131], v[44:47], v[72:75], v[128:131]\n"
    "ds_read_b128 v[92:95], v250 offset:53248\n"
    "v_fma_mix_f32 v17, v14, v32, v33 op_sel:[1,0,0] op_sel_hi:[1,0,0]\n"
    "v_fma_mix_f32 v18, v15, v34, v35 op_sel_hi:[1,0,0]\n"
    "v_mfma_f32_16x16x32_bf16 v[132:135], v[48:51], v[72:75], v[132:135]\n"
    "s_waitcnt lgkmcnt(4)\n"
    "v_fma_mix_f32 v19, v15, v34, v35 op_sel:[1,0,0] op_sel_hi:[1,0,0]\n"
    "v_cvt_pk_bf16_f32 v243, v16, v17\n"
    "v_mfma_f32_16x16x32_bf16 v[136:139], v[44:47], v[76:79], v[136:139]\n"
    "ds_read_b128 v[28:31], v4 offset:6144\n"
    "v_cvt_pk_bf16_f32 v247, v18, v19\n"
    "v_and_b32 v8, s51, v26\n"
    "v_mfma_f32_16x16x32_bf16 v[140:143], v[48:51], v[76:79], v[140:143]\n"
    "s_waitcnt lgkmcnt(4)\n"
    "ds_read2st64_b64 v[20:23], v254 offset0:160 offset1:161\n"
    "v_pk_lshrrev_b16 v9, 12, v26 op_sel_hi:[0,1]\n"
    "v_mfma_f32_16x16x32_bf16 v[144:147], v[44:47], v[80:83], v[144:147]\n"
    "ds_read_b128 v[52:55], v251 offset:53248\n"
    "v_fma_mix_f32 v10, v8, v32, v33 op_sel_hi:[1,0,0]\n"
    "v_fma_mix_f32 v11, v8, v32, v33 op_sel:[1,0,0] op_sel_hi:[1,0,0]\n"
    "v_mfma_f32_16x16x32_bf16 v[148:151], v[48:51], v[80:83], v[148:151]\n"
    "s_waitcnt lgkmcnt(5)\n"
    "ds_read_b128 v[56:59], v253 offset:12288\n"
    "v_fma_mix_f32 v12, v9, v34, v35 op_sel_hi:[1,0,0]\n"
    "v_fma_mix_f32 v13, v9, v34, v35 op_sel:[1,0,0] op_sel_hi:[1,0,0]\n"
    "v_mfma_f32_16x16x32_bf16 v[152:155], v[44:47], v[84:87], v[152:155]\n"
    "ds_read_b128 v[60:63], v253 offset:14336\n"
    "v_cvt_pk_bf16_f32 v244, v10, v11\n"
    "v_mfma_f32_16x16x32_bf16 v[156:159], v[48:51], v[84:87], v[156:159]\n"
    "s_waitcnt lgkmcnt(6)\n"
    "v_cvt_pk_bf16_f32 v248, v12, v13\n"
    "v_and_b32 v14, s51, v27\n"
    "v_mfma_f32_16x16x32_bf16 v[160:163], v[44:47], v[88:91], v[160:163]\n"
    "ds_read_b128 v[64:67], v253 offset:16384\n"
    "v_pk_lshrrev_b16 v15, 12, v27 op_sel_hi:[0,1]\n"
    "v_fma_mix_f32 v16, v14, v32, v33 op_sel_hi:[1,0,0]\n"
    "v_mfma_f32_16x16x32_bf16 v[164:167], v[48:51], v[88:91], v[164:167]\n"
    "s_waitcnt lgkmcnt(6)\n"
    "v_fma_mix_f32 v17, v14, v32, v33 op_sel:[1,0,0] op_sel_hi:[1,0,0]\n"
    "v_fma_mix_f32 v18, v15, v34, v35 op_sel_hi:[1,0,0]\n"
    "v_mfma_f32_16x16x32_bf16 v[168:171], v[36:39], v[92:95], v[168:171]\n"
    "ds_read_b128 v[68:71], v253 offset:18432\n"
    "v_fma_mix_f32 v19, v15, v34, v35 op_sel:[1,0,0] op_sel_hi:[1,0,0]\n"
    "v_mfma_f32_16x16x32_bf16 v[172:175], v[40:43], v[92:95], v[172:175]\n"
    "s_waitcnt lgkmcnt(4)\n"
    "v_cvt_pk_bf16_f32 v245, v16, v17\n"
    "v_cvt_pk_bf16_f32 v249, v18, v19\n"
    "v_mfma_f32_16x16x32_bf16 v[168:171], v[234:237], v[52:55], v[168:171]\n"
    "ds_read_b128 v[72:75], v253 offset:20480\n"
    "v_and_b32 v8, s51, v20\n"
    "v_pk_lshrrev_b16 v9, 12, v20 op_sel_hi:[0,1]\n"
    "v_mfma_f32_16x16x32_bf16 v[172:175], v[238:241], v[52:55], v[172:175]\n"
    "s_waitcnt lgkmcnt(4)\n"
    "v_fma_mix_f32 v10, v8, v28, v29 op_sel_hi:[1,0,0]\n"
    "v_fma_mix_f32 v11, v8, v28, v29 op_sel:[1,0,0] op_sel_hi:[1,0,0]\n"
    "v_mfma_f32_16x16x32_bf16 v[112:115], v[242:245], v[56:59], v[112:115]\n"
    "ds_read_b128 v[76:79], v253 offset:22528\n"
    "v_fma_mix_f32 v12, v9, v30, v31 op_sel_hi:[1,0,0]\n"
    "v_mfma_f32_16x16x32_bf16 v[116:119], v[246:249], v[56:59], v[116:119]\n"
    "s_waitcnt lgkmcnt(4)\n"
    "v_fma_mix_f32 v13, v9, v30, v31 op_sel:[1,0,0] op_sel_hi:[1,0,0]\n"
    "v_cvt_pk_bf16_f32 v96, v10, v11\n"
    "v_mfma_f32_16x16x32_bf16 v[120:123], v[242:245], v[60:63], v[120:123]\n"
    "ds_read_b128 v[80:83], v253 offset:24576\n"
    "v_cvt_pk_bf16_f32 v100, v12, v13\n"
    "v_and_b32 v14, s51, v21\n"
    "v_mfma_f32_16x16x32_bf16 v[124:127], v[246:249], v[60:63], v[124:127]\n"
    "s_waitcnt lgkmcnt(4)\n"
    "v_pk_lshrrev_b16 v15, 12, v21 op_sel_hi:[0,1]\n"
    "v_fma_mix_f32 v16, v14, v28, v29 op_sel_hi:[1,0,0]\n"
    "v_mfma_f32_16x16x32_bf16 v[128:131], v[242:245], v[64:67], v[128:131]\n"
    "ds_read_b128 v[84:87], v252 offset:26624\n"
    "v_fma_mix_f32 v17, v14, v28, v29 op_sel:[1,0,0] op_sel_hi:[1,0,0]\n"
    "v_mfma_f32_16x16x32_bf16 v[132:135], v[246:249], v[64:67], v[132:135]\n"
    "s_waitcnt lgkmcnt(4)\n"
    "v_fma_mix_f32 v18, v15, v30, v31 op_sel_hi:[1,0,0]\n"
    "v_fma_mix_f32 v19, v15, v30, v31 op_sel:[1,0,0] op_sel_hi:[1,0,0]\n"
    "v_mfma_f32_16x16x32_bf16 v[136:139], v[242:245], v[68:71], v[136:139]\n"
    "ds_read_b128 v[88:91], v253 offset:26624\n"
    "v_cvt_pk_bf16_f32 v97, v16, v17\n"
    "v_cvt_pk_bf16_f32 v101, v18, v19\n"
    "v_mfma_f32_16x16x32_bf16 v[140:143], v[246:249], v[68:71], v[140:143]\n"
    "s_waitcnt lgkmcnt(4)\n"
    "ds_read_b128 v[32:35], v4 offset:7168\n"
    "v_and_b32 v8, s51, v22\n"
    "v_mfma_f32_16x16x32_bf16 v[144:147], v[242:245], v[72:75], v[144:147]\n"
    "v_pk_lshrrev_b16 v9, 12, v22 op_sel_hi:[0,1]\n"
    "v_fma_mix_f32 v10, v8, v28, v29 op_sel_hi:[1,0,0]\n"
    "v_mfma_f32_16x16x32_bf16 v[148:151], v[246:249], v[72:75], v[148:151]\n"
    "s_waitcnt lgkmcnt(4)\n"
    "v_fma_mix_f32 v11, v8, v28, v29 op_sel:[1,0,0] op_sel_hi:[1,0,0]\n"
    "v_fma_mix_f32 v12, v9, v30, v31 op_sel_hi:[1,0,0]\n"
    "v_mfma_f32_16x16x32_bf16 v[152:155], v[242:245], v[76:79], v[152:155]\n"
    "s_barrier\n"
    "ds_read2st64_b64 v[24:27], v254 offset0:0 offset1:1\n"
    "v_fma_mix_f32 v13, v9, v30, v31 op_sel:[1,0,0] op_sel_hi:[1,0,0]\n"
    "v_cvt_pk_bf16_f32 v98, v10, v11\n"
    "v_mfma_f32_16x16x32_bf16 v[156:159], v[246:249], v[76:79], v[156:159]\n"
    "s_waitcnt lgkmcnt(4)\n"
    "ds_read_b128 v[92:95], v250 offset:0\n"
    "v_cvt_pk_bf16_f32 v102, v12, v13\n"
    "v_mfma_f32_16x16x32_bf16 v[160:163], v[242:245], v[80:83], v[160:163]\n"
    "ds_read_b128 v[52:55], v250 offset:2048\n"
    "v_and_b32 v14, s51, v23\n"
    "v_pk_lshrrev_b16 v15, 12, v23 op_sel_hi:[0,1]\n"
    "v_mfma_f32_16x16x32_bf16 v[164:167], v[246:249], v[80:83], v[164:167]\n"
    "s_waitcnt lgkmcnt(5)\n"
    "ds_read_b128 v[56:59], v250 offset:4096\n"
    "v_fma_mix_f32 v16, v14, v28, v29 op_sel_hi:[1,0,0]\n"
    "v_mfma_f32_16x16x32_bf16 v[168:171], v[44:47], v[84:87], v[168:171]\n"
    "ds_read_b128 v[60:63], v250 offset:6144\n"
    "v_fma_mix_f32 v17, v14, v28, v29 op_sel:[1,0,0] op_sel_hi:[1,0,0]\n"
    "v_fma_mix_f32 v18, v15, v30, v31 op_sel_hi:[1,0,0]\n"
    "v_mfma_f32_16x16x32_bf16 v[172:175], v[48:51], v[84:87], v[172:175]\n"
    "s_waitcnt lgkmcnt(5)\n"
    "v_fma_mix_f32 v19, v15, v30, v31 op_sel:[1,0,0] op_sel_hi:[1,0,0]\n"
    "v_cvt_pk_bf16_f32 v99, v16, v17\n"
    "v_mfma_f32_16x16x32_bf16 v[168:171], v[242:245], v[88:91], v[168:171]\n"
    "ds_read_b128 v[64:67], v250 offset:8192\n"
    "v_cvt_pk_bf16_f32 v103, v18, v19\n"
    "v_mfma_f32_16x16x32_bf16 v[172:175], v[246:249], v[88:91], v[172:175]\n"
    "s_waitcnt lgkmcnt(4)\n"
    "v_and_b32 v8, s51, v24\n"
    "v_pk_lshrrev_b16 v9, 12, v24 op_sel_hi:[0,1]\n"
    "v_fma_mix_f32 v10, v8, v32, v33 op_sel_hi:[1,0,0]\n"
    "v_mfma_f32_16x16x32_bf16 v[112:115], v[96:99], v[92:95], v[112:115]\n"
    "ds_read_b128 v[68:71], v250 offset:10240\n"
    "v_fma_mix_f32 v11, v8, v32, v33 op_sel:[1,0,0] op_sel_hi:[1,0,0]\n"
    "v_fma_mix_f32 v12, v9, v34, v35 op_sel_hi:[1,0,0]\n"
    "v_mfma_f32_16x16x32_bf16 v[116:119], v[100:103], v[92:95], v[116:119]\n"
    "s_waitcnt lgkmcnt(4)\n"
    "v_fma_mix_f32 v13, v9, v34, v35 op_sel:[1,0,0] op_sel_hi:[1,0,0]\n"
    "v_cvt_pk_bf16_f32 v104, v10, v11\n"
    "v_mfma_f32_16x16x32_bf16 v[120:123], v[96:99], v[52:55], v[120:123]\n"
    "ds_read_b128 v[72:75], v250 offset:12288\n"
    "v_cvt_pk_bf16_f32 v108, v12, v13\n"
    "v_and_b32 v14, s51, v25\n"
    "v_pk_lshrrev_b16 v15, 12, v25 op_sel_hi:[0,1]\n"
    "v_mfma_f32_16x16x32_bf16 v[124:127], v[100:103], v[52:55], v[124:127]\n"
    "s_waitcnt lgkmcnt(4)\n"
    "v_fma_mix_f32 v16, v14, v32, v33 op_sel_hi:[1,0,0]\n"
    "v_fma_mix_f32 v17, v14, v32, v33 op_sel:[1,0,0] op_sel_hi:[1,0,0]\n"
    "v_mfma_f32_16x16x32_bf16 v[128:131], v[96:99], v[56:59], v[128:131]\n"
    "ds_read_b128 v[76:79], v251 offset:0\n"
    "v_fma_mix_f32 v18, v15, v34, v35 op_sel_hi:[1,0,0]\n"
    "v_fma_mix_f32 v19, v15, v34, v35 op_sel:[1,0,0] op_sel_hi:[1,0,0]\n"
    "v_mfma_f32_16x16x32_bf16 v[132:135], v[100:103], v[56:59], v[132:135]\n"
    "s_waitcnt lgkmcnt(4)\n"
    "v_cvt_pk_bf16_f32 v105, v16, v17\n"
    "v_cvt_pk_bf16_f32 v109, v18, v19\n"
    "v_mfma_f32_16x16x32_bf16 v[136:139], v[96:99], v[60:63], v[136:139]\n"
    "ds_read_b128 v[80:83], v251 offset:2048\n"
    "v_and_b32 v8, s51, v26\n"
    "v_pk_lshrrev_b16 v9, 12, v26 op_sel_hi:[0,1]\n"
    "v_fma_mix_f32 v10, v8, v32, v33 op_sel_hi:[1,0,0]\n"
    "v_mfma_f32_16x16x32_bf16 v[140:143], v[100:103], v[60:63], v[140:143]\n"
    "s_waitcnt lgkmcnt(4)\n"
    "v_fma_mix_f32 v11, v8, v32, v33 op_sel:[1,0,0] op_sel_hi:[1,0,0]\n"
    "v_fma_mix_f32 v12, v9, v34, v35 op_sel_hi:[1,0,0]\n"
    "v_mfma_f32_16x16x32_bf16 v[144:147], v[96:99], v[64:67], v[144:147]\n"
    "ds_read_b128 v[84:87], v251 offset:4096\n"
    "v_fma_mix_f32 v13, v9, v34, v35 op_sel:[1,0,0] op_sel_hi:[1,0,0]\n"
    "v_cvt_pk_bf16_f32 v106, v10, v11\n"
    "v_mfma_f32_16x16x32_bf16 v[148:151], v[100:103], v[64:67], v[148:151]\n"
    "s_waitcnt lgkmcnt(4)\n"
    "v_cvt_pk_bf16_f32 v110, v12, v13\n"
    "v_and_b32 v14, s51, v27\n"
    "v_pk_lshrrev_b16 v15, 12, v27 op_sel_hi:[0,1]\n"
    "v_mfma_f32_16x16x32_bf16 v[152:155], v[96:99], v[68:71], v[152:155]\n"
    "ds_read_b128 v[88:91], v251 offset:6144\n"
    "v_fma_mix_f32 v16, v14, v32, v33 op_sel_hi:[1,0,0]\n"
    "v_fma_mix_f32 v17, v14, v32, v33 op_sel:[1,0,0] op_sel_hi:[1,0,0]\n"
    "v_mfma_f32_16x16x32_bf16 v[156:159], v[100:103], v[68:71], v[156:159]\n"
    "s_waitcnt lgkmcnt(4)\n"
    "v_fma_mix_f32 v18, v15, v34, v35 op_sel_hi:[1,0,0]\n"
    "v_fma_mix_f32 v19, v15, v34, v35 op_sel:[1,0,0] op_sel_hi:[1,0,0]\n"
    "v_mfma_f32_16x16x32_bf16 v[160:163], v[96:99], v[72:75], v[160:163]\n"
    "ds_read_b128 v[92:95], v251 offset:8192\n"
    "v_cvt_pk_bf16_f32 v107, v16, v17\n"
    "v_cvt_pk_bf16_f32 v111, v18, v19\n"
    "v_mfma_f32_16x16x32_bf16 v[164:167], v[100:103], v[72:75], v[164:167]\n"
    "s_waitcnt lgkmcnt(4)\n"
    "v_mfma_f32_16x16x32_bf16 v[112:115], v[104:107], v[76:79], v[112:115]\n"
    "151:\n"
    "s_cbranch_execz 41b\n"
    "ds_read_b128 v[52:55], v251 offset:10240\n"
    "v_mfma_f32_16x16x32_bf16 v[116:119], v[108:111], v[76:79], v[116:119]\n"
    "s_waitcnt lgkmcnt(4)\n"
    "v_mfma_f32_16x16x32_bf16 v[120:123], v[104:107], v[80:83], v[120:123]\n"
    "ds_read_b128 v[56:59], v251 offset:12288\n"
    "v_mfma_f32_16x16x32_bf16 v[124:127], v[108:111], v[80:83], v[124:127]\n"
    "s_waitcnt lgkmcnt(4)\n"
    "v_mfma_f32_16x16x32_bf16 v[128:131], v[104:107], v[84:87], v[128:131]\n"
    "ds_read_b128 v[60:63], v250 offset:14336\n"
    "s_nop 0\n"
    "v_cvt_pk_bf16_f32 v8, v112, v116\n"
    "v_mfma_f32_16x16x32_bf16 v[132:135], v[108:111], v[84:87], v[132:135]\n"
    "s_waitcnt lgkmcnt(4)\n"
    "v_cvt_pk_bf16_f32 v9, v113, v117\n"
    "v_cvt_pk_bf16_f32 v10, v114, v118\n"
    "v_mfma_f32_16x16x32_bf16 v[136:139], v[104:107], v[88:91], v[136:139]\n"
    "ds_read_b128 v[64:67], v251 offset:14336\n"
    "v_cvt_pk_bf16_f32 v11, v115, v119\n"
    "v_mfma_f32_16x16x32_bf16 v[140:143], v[108:111], v[88:91], v[140:143]\n"
    "s_waitcnt lgkmcnt(4)\n"
    "ds_write_b128 v232, v[8:11] offset:8192\n"
    "v_cvt_pk_bf16_f32 v12, v120, v124\n"
    "v_mfma_f32_16x16x32_bf16 v[144:147], v[104:107], v[92:95], v[144:147]\n"
    "ds_read_b128 v[68:71], v250 offset:55296\n"
    "v_cvt_pk_bf16_f32 v13, v121, v125\n"
    "v_mfma_f32_16x16x32_bf16 v[148:151], v[108:111], v[92:95], v[148:151]\n"
    "s_waitcnt lgkmcnt(5)\n"
    "v_cvt_pk_bf16_f32 v14, v122, v126\n"
    "v_cvt_pk_bf16_f32 v15, v123, v127\n"
    "v_mfma_f32_16x16x32_bf16 v[152:155], v[104:107], v[52:55], v[152:155]\n"
    "ds_read_b128 v[72:75], v251 offset:55296\n"
    "ds_write_b128 v232, v[12:15] offset:12288\n"
    "v_mfma_f32_16x16x32_bf16 v[156:159], v[108:111], v[52:55], v[156:159]\n"
    "s_waitcnt lgkmcnt(6)\n"
    "v_cvt_pk_bf16_f32 v16, v128, v132\n"
    "v_cvt_pk_bf16_f32 v17, v129, v133\n"
    "v_mfma_f32_16x16x32_bf16 v[160:163], v[104:107], v[56:59], v[160:163]\n"
    "ds_read_b128 v[76:79], v252 offset:28672\n"
    "v_cvt_pk_bf16_f32 v18, v130, v134\n"
    "v_mfma_f32_16x16x32_bf16 v[164:167], v[108:111], v[56:59], v[164:167]\n"
    "s_waitcnt lgkmcnt(6)\n"
    "s_waitcnt lgkmcnt(1)\n"
    "s_barrier\n"
    "v_cvt_pk_bf16_f32 v19, v131, v135\n"
    "ds_write_b128 v232, v[16:19] offset:16384\n"
    "v_mfma_f32_16x16x32_bf16 v[168:171], v[96:99], v[60:63], v[168:171]\n"
    "ds_read_b128 v[80:83], v253 offset:28672\n"
    "v_cvt_pk_bf16_f32 v20, v136, v140\n"
    "v_mfma_f32_16x16x32_bf16 v[172:175], v[100:103], v[60:63], v[172:175]\n"
    "v_cvt_pk_bf16_f32 v21, v137, v141\n"
    "v_cvt_pk_bf16_f32 v22, v138, v142\n"
    "v_mfma_f32_16x16x32_bf16 v[168:171], v[104:107], v[64:67], v[168:171]\n"
    "ds_read_b128 v[84:87], v250 offset:16384\n"
    "v_cvt_pk_bf16_f32 v23, v139, v143\n"
    "v_mfma_f32_16x16x32_bf16 v[172:175], v[108:111], v[64:67], v[172:175]\n"
    "ds_write_b128 v232, v[20:23] offset:20480\n"
    "v_cvt_pk_bf16_f32 v24, v144, v148\n"
    "v_mfma_f32_16x16x32_bf16 v[176:179], v[36:39], v[68:71], v[176:179]\n"
    "ds_read_b128 v[88:91], v251 offset:16384\n"
    "v_cvt_pk_bf16_f32 v25, v145, v149\n"
    "v_mfma_f32_16x16x32_bf16 v[180:183], v[40:43], v[68:71], v[180:183]\n"
    "v_cvt_pk_bf16_f32 v26, v146, v150\n"
    "v_cvt_pk_bf16_f32 v27, v147, v151\n"
    "v_mfma_f32_16x16x32_bf16 v[176:179], v[234:237], v[72:75], v[176:179]\n"
    "ds_read_b128 v[92:95], v250 offset:57344\n"
    "ds_write_b128 v232, v[24:27] offset:24576\n"
    "v_mfma_f32_16x16x32_bf16 v[180:183], v[238:241], v[72:75], v[180:183]\n"
    "s_waitcnt lgkmcnt(6)\n"
    "v_cvt_pk_bf16_f32 v28, v152, v156\n"
    "v_cvt_pk_bf16_f32 v29, v153, v157\n"
    "v_mfma_f32_16x16x32_bf16 v[176:179], v[44:47], v[76:79], v[176:179]\n"
    "ds_read_b128 v[52:55], v251 offset:57344\n"
    "v_cvt_pk_bf16_f32 v30, v154, v158\n"
    "v_mfma_f32_16x16x32_bf16 v[180:183], v[48:51], v[76:79], v[180:183]\n"
    "s_waitcnt lgkmcnt(6)\n"
    "v_cvt_pk_bf16_f32 v31, v155, v159\n"
    "ds_write_b128 v232, v[28:31] offset:28672\n"
    "v_mfma_f32_16x16x32_bf16 v[176:179], v[242:245], v[80:83], v[176:179]\n"
    "ds_read_b128 v[56:59], v252 offset:30720\n"
    "v_cvt_pk_bf16_f32 v32, v160, v164\n"
    "v_mfma_f32_16x16x32_bf16 v[180:183], v[246:249], v[80:83], v[180:183]\n"
    "s_waitcnt lgkmcnt(6)\n"
    "v_cvt_pk_bf16_f32 v33, v161, v165\n"
    "v_cvt_pk_bf16_f32 v34, v162, v166\n"
    "v_mfma_f32_16x16x32_bf16 v[176:179], v[96:99], v[84:87], v[176:179]\n"
    "s_waitcnt lgkmcnt(1)\n"
    "s_barrier\n"
    "ds_read_b128 v[60:63], v253 offset:30720\n"
    "v_cvt_pk_bf16_f32 v35, v163, v167\n"
    "v_mfma_f32_16x16x32_bf16 v[180:183], v[100:103], v[84:87], v[180:183]\n"
    "ds_write_b128 v232, v[32:35] offset:40960\n"
    "v_cvt_pk_bf16_f32 v8, v168, v172\n"
    "v_mfma_f32_16x16x32_bf16 v[176:179], v[104:107], v[88:91], v[176:179]\n"
    "ds_read_b128 v[64:67], v250 offset:18432\n"
    "v_cvt_pk_bf16_f32 v9, v169, v173\n"
    "v_mfma_f32_16x16x32_bf16 v[180:183], v[108:111], v[88:91], v[180:183]\n"
    "v_cvt_pk_bf16_f32 v10, v170, v174\n"
    "v_cvt_pk_bf16_f32 v11, v171, v175\n"
    "v_mfma_f32_16x16x32_bf16 v[184:187], v[36:39], v[92:95], v[184:187]\n"
    "ds_read_b128 v[68:71], v251 offset:18432\n"
    "ds_write_b128 v232, v[8:11] offset:32768\n"
    "v_mfma_f32_16x16x32_bf16 v[188:191], v[40:43], v[92:95], v[188:191]\n"
    "v_mfma_f32_16x16x32_bf16 v[184:187], v[234:237], v[52:55], v[184:187]\n"
    "ds_read_b128 v[72:75], v250 offset:59392\n"
    "v_cvt_pk_bf16_f32 v12, v176, v180\n"
    "v_mfma_f32_16x16x32_bf16 v[188:191], v[238:241], v[52:55], v[188:191]\n"
    "s_waitcnt lgkmcnt(6)\n"
    "v_cvt_pk_bf16_f32 v13, v177, v181\n"
    "v_cvt_pk_bf16_f32 v14, v178, v182\n"
    "v_mfma_f32_16x16x32_bf16 v[184:187], v[44:47], v[56:59], v[184:187]\n"
    "ds_read_b128 v[76:79], v251 offset:59392\n"
    "v_cvt_pk_bf16_f32 v15, v179, v183\n"
    "v_mfma_f32_16x16x32_bf16 v[188:191], v[48:51], v[56:59], v[188:191]\n"
    "s_waitcnt lgkmcnt(5)\n"
    "ds_write_b128 v232, v[12:15] offset:36864\n"
    "v_mfma_f32_16x16x32_bf16 v[184:187], v[242:245], v[60:63], v[184:187]\n"
    "ds_read_b128 v[80:83], v252 offset:32768\n"
    "v_mfma_f32_16x16x32_bf16 v[188:191], v[246:249], v[60:63], v[188:191]\n"
    "s_waitcnt lgkmcnt(6)\n"
    "v_mfma_f32_16x16x32_bf16 v[184:187], v[96:99], v[64:67], v[184:187]\n"
    "ds_read_b128 v[84:87], v253 offset:32768\n"
    "v_mfma_f32_16x16x32_bf16 v[188:191], v[100:103], v[64:67], v[188:191]\n"
    "s_waitcnt lgkmcnt(5)\n"
    "v_mfma_f32_16x16x32_bf16 v[184:187], v[104:107], v[68:71], v[184:187]\n"
    "ds_read_b128 v[88:91], v250 offset:20480\n"
    "v_mfma_f32_16x16x32_bf16 v[188:191], v[108:111], v[68:71], v[188:191]\n"
    "s_waitcnt lgkmcnt(5)\n"
    "v_mfma_f32_16x16x32_bf16 v[192:195], v[36:39], v[72:75], v[192:195]\n"
    "ds_read_b128 v[92:95], v251 offset:20480\n"
    "v_mfma_f32_16x16x32_bf16 v[196:199], v[40:43], v[72:75], v[196:199]\n"
    "s_waitcnt lgkmcnt(4)\n"
    "v_mfma_f32_16x16x32_bf16 v[192:195], v[234:237], v[76:79], v[192:195]\n"
    "ds_read_b128 v[52:55], v250 offset:61440\n"
    "s_nop 0\n"
    "v_cvt_pk_bf16_f32 v16, v184, v188\n"
    "v_mfma_f32_16x16x32_bf16 v[196:199], v[238:241], v[76:79], v[196:199]\n"
    "s_waitcnt lgkmcnt(4)\n"
    "v_cvt_pk_bf16_f32 v17, v185, v189\n"
    "v_cvt_pk_bf16_f32 v18, v186, v190\n"
    "v_mfma_f32_16x16x32_bf16 v[192:195], v[44:47], v[80:83], v[192:195]\n"
    "ds_read_b128 v[56:59], v251 offset:61440\n"
    "v_cvt_pk_bf16_f32 v19, v187, v191\n"
    "v_mfma_f32_16x16x32_bf16 v[196:199], v[48:51], v[80:83], v[196:199]\n"
    "s_waitcnt lgkmcnt(4)\n"
    "ds_write_b128 v232, v[16:19] offset:0\n"
    "v_mfma_f32_16x16x32_bf16 v[192:195], v[242:245], v[84:87], v[192:195]\n"
    "ds_read_b128 v[60:63], v252 offset:34816\n"
    "v_mfma_f32_16x16x32_bf16 v[196:199], v[246:249], v[84:87], v[196:199]\n"
    "s_waitcnt lgkmcnt(5)\n"
    "v_mfma_f32_16x16x32_bf16 v[192:195], v[96:99], v[88:91], v[192:195]\n"
    "s_waitcnt lgkmcnt(1)\n"
    "s_barrier\n"
    "ds_read_b128 v[64:67], v253 offset:34816\n"
    "v_mfma_f32_16x16x32_bf16 v[196:199], v[100:103], v[88:91], v[196:199]\n"
    "v_mfma_f32_16x16x32_bf16 v[192:195], v[104:107], v[92:95], v[192:195]\n"
    "ds_read_b128 v[68:71], v250 offset:22528\n"
    "v_mfma_f32_16x16x32_bf16 v[196:199], v[108:111], v[92:95], v[196:199]\n"
    "v_mfma_f32_16x16x32_bf16 v[200:203], v[36:39], v[52:55], v[200:203]\n"
    "ds_read_b128 v[72:75], v251 offset:22528\n"
    "v_mfma_f32_16x16x32_bf16 v[204:207], v[40:43], v[52:55], v[204:207]\n"
    "v_mfma_f32_16x16x32_bf16 v[200:203], v[234:237], v[56:59], v[200:203]\n"
    "ds_read_b128 v[76:79], v250 offset:63488\n"
    "s_nop 2\n"
    "v_cvt_pk_bf16_f32 v20, v192, v196\n"
    "v_mfma_f32_16x16x32_bf16 v[204:207], v[238:241], v[56:59], v[204:207]\n"
    "s_waitcnt lgkmcnt(4)\n"
    "v_cvt_pk_bf16_f32 v21, v193, v197\n"
    "v_cvt_pk_bf16_f32 v22, v194, v198\n"
    "v_mfma_f32_16x16x32_bf16 v[200:203], v[44:47], v[60:63], v[200:203]\n"
    "ds_read_b128 v[80:83], v251 offset:63488\n"
    "v_cvt_pk_bf16_f32 v23, v195, v199\n"
    "v_mfma_f32_16x16x32_bf16 v[204:207], v[48:51], v[60:63], v[204:207]\n"
    "s_waitcnt lgkmcnt(4)\n"
    "ds_write_b128 v232, v[20:23] offset:4096\n"
    "v_mfma_f32_16x16x32_bf16 v[200:203], v[242:245], v[64:67], v[200:203]\n"
    "ds_read_b128 v[84:87], v252 offset:36864\n"
    "v_mfma_f32_16x16x32_bf16 v[204:207], v[246:249], v[64:67], v[204:207]\n"
    "s_waitcnt lgkmcnt(5)\n"
    "v_mfma_f32_16x16x32_bf16 v[200:203], v[96:99], v[68:71], v[200:203]\n"
    "ds_read_b128 v[88:91], v253 offset:36864\n"
    "v_mfma_f32_16x16x32_bf16 v[204:207], v[100:103], v[68:71], v[204:207]\n"
    "s_waitcnt lgkmcnt(5)\n"
    "v_mfma_f32_16x16x32_bf16 v[200:203], v[104:107], v[72:75], v[200:203]\n"
    "ds_read_b128 v[92:95], v250 offset:24576\n"
    "v_mfma_f32_16x16x32_bf16 v[204:207], v[108:111], v[72:75], v[204:207]\n"
    "s_waitcnt lgkmcnt(5)\n"
    "v_mfma_f32_16x16x32_bf16 v[208:211], v[36:39], v[76:79], v[208:211]\n"
    "ds_read_b128 v[52:55], v251 offset:24576\n"
    "v_mfma_f32_16x16x32_bf16 v[212:215], v[40:43], v[76:79], v[212:215]\n"
    "s_waitcnt lgkmcnt(4)\n"
    "v_mfma_f32_16x16x32_bf16 v[208:211], v[234:237], v[80:83], v[208:211]\n"
    "ds_read_b128 v[56:59], v252 offset:0\n"
    "s_nop 0\n"
    "v_cvt_pk_bf16_f32 v24, v200, v204\n"
    "v_mfma_f32_16x16x32_bf16 v[212:215], v[238:241], v[80:83], v[212:215]\n"
    "s_waitcnt lgkmcnt(4)\n"
    "v_cvt_pk_bf16_f32 v25, v201, v205\n"
    "v_cvt_pk_bf16_f32 v26, v202, v206\n"
    "v_mfma_f32_16x16x32_bf16 v[208:211], v[44:47], v[84:87], v[208:211]\n"
    "ds_read_b128 v[60:63], v253 offset:0\n"
    "v_cvt_pk_bf16_f32 v27, v203, v207\n"
    "v_mfma_f32_16x16x32_bf16 v[212:215], v[48:51], v[84:87], v[212:215]\n"
    "s_waitcnt lgkmcnt(4)\n"
    "ds_write_b128 v233, v[24:27] offset:38912\n"
    "v_mfma_f32_16x16x32_bf16 v[208:211], v[242:245], v[88:91], v[208:211]\n"
    "ds_read_b128 v[64:67], v252 offset:38912\n"
    "v_mfma_f32_16x16x32_bf16 v[212:215], v[246:249], v[88:91], v[212:215]\n"
    "s_waitcnt lgkmcnt(5)\n"
    "v_mfma_f32_16x16x32_bf16 v[208:211], v[96:99], v[92:95], v[208:211]\n"
    "ds_read_b128 v[68:71], v253 offset:38912\n"
    "v_mfma_f32_16x16x32_bf16 v[212:215], v[100:103], v[92:95], v[212:215]\n"
    "s_waitcnt lgkmcnt(5)\n"
    "v_mfma_f32_16x16x32_bf16 v[208:211], v[104:107], v[52:55], v[208:211]\n"
    "ds_read_b128 v[72:75], v250 offset:26624\n"
    "v_mfma_f32_16x16x32_bf16 v[212:215], v[108:111], v[52:55], v[212:215]\n"
    "s_waitcnt lgkmcnt(5)\n"
    "v_mfma_f32_16x16x32_bf16 v[216:219], v[36:39], v[56:59], v[216:219]\n"
    "ds_read_b128 v[76:79], v251 offset:26624\n"
    "v_mfma_f32_16x16x32_bf16 v[220:223], v[40:43], v[56:59], v[220:223]\n"
    "s_waitcnt lgkmcnt(4)\n"
    "v_mfma_f32_16x16x32_bf16 v[216:219], v[234:237], v[60:63], v[216:219]\n"
    "ds_read_b128 v[80:83], v252 offset:2048\n"
    "s_nop 0\n"
    "v_cvt_pk_bf16_f32 v28, v208, v212\n"
    "v_mfma_f32_16x16x32_bf16 v[220:223], v[238:241], v[60:63], v[220:223]\n"
    "s_waitcnt lgkmcnt(4)\n"
    "v_cvt_pk_bf16_f32 v29, v209, v213\n"
    "v_cvt_pk_bf16_f32 v30, v210, v214\n"
    "v_mfma_f32_16x16x32_bf16 v[216:219], v[44:47], v[64:67], v[216:219]\n"
    "ds_read_b128 v[84:87], v253 offset:2048\n"
    "v_cvt_pk_bf16_f32 v31, v211, v215\n"
    "v_mfma_f32_16x16x32_bf16 v[220:223], v[48:51], v[64:67], v[220:223]\n"
    "s_waitcnt lgkmcnt(4)\n"
    "ds_write_b128 v233, v[28:31] offset:43008\n"
    "v_mfma_f32_16x16x32_bf16 v[216:219], v[242:245], v[68:71], v[216:219]\n"
    "ds_read_b128 v[88:91], v252 offset:40960\n"
    "v_mfma_f32_16x16x32_bf16 v[220:223], v[246:249], v[68:71], v[220:223]\n"
    "s_waitcnt lgkmcnt(5)\n"
    "v_mfma_f32_16x16x32_bf16 v[216:219], v[96:99], v[72:75], v[216:219]\n"
    "ds_read_b128 v[92:95], v253 offset:40960\n"
    "v_mfma_f32_16x16x32_bf16 v[220:223], v[100:103], v[72:75], v[220:223]\n"
    "s_waitcnt lgkmcnt(5)\n"
    "v_mfma_f32_16x16x32_bf16 v[216:219], v[104:107], v[76:79], v[216:219]\n"
    "ds_read_b128 v[52:55], v250 offset:28672\n"
    "v_mfma_f32_16x16x32_bf16 v[220:223], v[108:111], v[76:79], v[220:223]\n"
    "s_waitcnt lgkmcnt(5)\n"
    "v_mfma_f32_16x16x32_bf16 v[224:227], v[36:39], v[80:83], v[224:227]\n"
    "ds_read_b128 v[56:59], v251 offset:28672\n"
    "v_mfma_f32_16x16x32_bf16 v[228:231], v[40:43], v[80:83], v[228:231]\n"
    "s_waitcnt lgkmcnt(4)\n"
    "v_mfma_f32_16x16x32_bf16 v[224:227], v[234:237], v[84:87], v[224:227]\n"
    "s_nop 1\n"
    "v_cvt_pk_bf16_f32 v32, v216, v220\n"
    "v_cvt_pk_bf16_f32 v33, v217, v221\n"
    "v_mfma_f32_16x16x32_bf16 v[228:231], v[238:241], v[84:87], v[228:231]\n"
    "s_waitcnt lgkmcnt(3)\n"
    "v_cvt_pk_bf16_f32 v34, v218, v222\n"
    "v_cvt_pk_bf16_f32 v35, v219, v223\n"
    "v_mfma_f32_16x16x32_bf16 v[224:227], v[44:47], v[88:91], v[224:227]\n"
    "ds_write_b128 v233, v[32:35] offset:0\n"
    "v_mfma_f32_16x16x32_bf16 v[228:231], v[48:51], v[88:91], v[228:231]\n"
    "s_waitcnt lgkmcnt(3)\n"
    "v_mfma_f32_16x16x32_bf16 v[224:227], v[242:245], v[92:95], v[224:227]\n"
    "v_mfma_f32_16x16x32_bf16 v[228:231], v[246:249], v[92:95], v[228:231]\n"
    "s_waitcnt lgkmcnt(2)\n"
    "v_mfma_f32_16x16x32_bf16 v[224:227], v[96:99], v[52:55], v[224:227]\n"
    "v_mfma_f32_16x16x32_bf16 v[228:231], v[100:103], v[52:55], v[228:231]\n"
    "s_waitcnt lgkmcnt(1)\n"
    "v_mfma_f32_16x16x32_bf16 v[224:227], v[104:107], v[56:59], v[224:227]\n"
    "v_mfma_f32_16x16x32_bf16 v[228:231], v[108:111], v[56:59], v[228:231]\n"
    "s_nop 7\n"
    "v_cvt_pk_bf16_f32 v8, v224, v228\n"
    "v_cvt_pk_bf16_f32 v9, v225, v229\n"
    "v_cvt_pk_bf16_f32 v10, v226, v230\n"
    "v_cvt_pk_bf16_f32 v11, v227, v231\n"
    "ds_write_b128 v233, v[8:11] offset:4096\n"
    "s_waitcnt lgkmcnt(0)\n"
    "s_barrier\n"
    "s_endpgm\n"
    "152:\n"
    "s_branch 41b\n"
    ".p2align 3\n"
    "20:\n"
    "v_mfma_f32_16x16x32_bf16 v[112:115], v[44:47], v[52:55], v[112:115]\n"
    "ds_read_b128 v[104:107], v2 offset:26624\n"
    "v_and_b32 v8, s51, v20\n"
    "v_mfma_f32_16x16x32_bf16 v[116:119], v[48:51], v[52:55], v[116:119]\n"
    "v_pk_lshrrev_b16 v9, 12, v20 op_sel_hi:[0,1]\n"
    "v_fma_mix_f32 v10, v8, v28, v29 op_sel_hi:[1,0,0]\n"
    "v_mfma_f32_16x16x32_bf16 v[120:123], v[44:47], v[56:59], v[120:123]\n"
    "v_fma_mix_f32 v11, v8, v28, v29 op_sel:[1,0,0] op_sel_hi:[1,0,0]\n"
    "v_mfma_f32_16x16x32_bf16 v[124:127], v[48:51], v[56:59], v[124:127]\n"
    "v_fma_mix_f32 v12, v9, v30, v31 op_sel_hi:[1,0,0]\n"
    "v_fma_mix_f32 v13, v9, v30, v31 op_sel:[1,0,0] op_sel_hi:[1,0,0]\n"
    "v_mfma_f32_16x16x32_bf16 v[128:131], v[44:47], v[60:63], v[128:131]\n"
    "v_cvt_pk_bf16_f32 v36, v10, v11\n"
    "v_mfma_f32_16x16x32_bf16 v[132:135], v[48:51], v[60:63], v[132:135]\n"
    "s_waitcnt lgkmcnt(0)\n"
    "v_cvt_pk_bf16_f32 v40, v12, v13\n"
    "v_and_b32 v14, s51, v21\n"
    "v_mfma_f32_16x16x32_bf16 v[136:139], v[44:47], v[64:67], v[136:139]\n"
    "s_barrier\n"
    "ds_read_b128 v[52:55], v1 offset:0\n"
    "v_pk_lshrrev_b16 v15, 12, v21 op_sel_hi:[0,1]\n"
    "v_mfma_f32_16x16x32_bf16 v[140:143], v[48:51], v[64:67], v[140:143]\n"
    "ds_read2st64_b64 v[24:27], v3 offset0:0 offset1:1\n"
    "v_fma_mix_f32 v16, v14, v28, v29 op_sel_hi:[1,0,0]\n"
    "v_mfma_f32_16x16x32_bf16 v[144:147], v[44:47], v[68:71], v[144:147]\n"
    "ds_read_b128 v[56:59], v1 offset:2048\n"
    "v_fma_mix_f32 v17, v14, v28, v29 op_sel:[1,0,0] op_sel_hi:[1,0,0]\n"
    "v_mfma_f32_16x16x32_bf16 v[148:151], v[48:51], v[68:71], v[148:151]\n"
    "ds_read_b128 v[32:35], v4 offset:3072\n"
    "v_fma_mix_f32 v18, v15, v30, v31 op_sel_hi:[1,0,0]\n"
    "v_mfma_f32_16x16x32_bf16 v[152:155], v[44:47], v[72:75], v[152:155]\n"
    "ds_read_b128 v[60:63], v1 offset:4096\n"
    "v_fma_mix_f32 v19, v15, v30, v31 op_sel:[1,0,0] op_sel_hi:[1,0,0]\n"
    "v_mfma_f32_16x16x32_bf16 v[156:159], v[48:51], v[72:75], v[156:159]\n"
    "ds_read_b128 v[64:67], v1 offset:6144\n"
    "v_cvt_pk_bf16_f32 v37, v16, v17\n"
    "v_mfma_f32_16x16x32_bf16 v[160:163], v[44:47], v[76:79], v[160:163]\n"
    "ds_read_b128 v[68:71], v1 offset:8192\n"
    "v_cvt_pk_bf16_f32 v41, v18, v19\n"
    "v_mfma_f32_16x16x32_bf16 v[164:167], v[48:51], v[76:79], v[164:167]\n"
    "ds_read_b128 v[72:75], v1 offset:10240\n"
    "v_and_b32 v8, s51, v22\n"
    "v_mfma_f32_16x16x32_bf16 v[168:171], v[44:47], v[80:83], v[168:171]\n"
    "ds_read_b128 v[76:79], v1 offset:12288\n"
    "v_pk_lshrrev_b16 v9, 12, v22 op_sel_hi:[0,1]\n"
    "v_mfma_f32_16x16x32_bf16 v[172:175], v[48:51], v[80:83], v[172:175]\n"
    "v_fma_mix_f32 v10, v8, v28, v29 op_sel_hi:[1,0,0]\n"
    "v_mfma_f32_16x16x32_bf16 v[176:179], v[44:47], v[84:87], v[176:179]\n"
    "ds_read_b128 v[80:83], v1 offset:14336\n"
    "v_fma_mix_f32 v11, v8, v28, v29 op_sel:[1,0,0] op_sel_hi:[1,0,0]\n"
    "v_mfma_f32_16x16x32_bf16 v[180:183], v[48:51], v[84:87], v[180:183]\n"
    "v_fma_mix_f32 v12, v9, v30, v31 op_sel_hi:[1,0,0]\n"
    "v_fma_mix_f32 v13, v9, v30, v31 op_sel:[1,0,0] op_sel_hi:[1,0,0]\n"
    "v_mfma_f32_16x16x32_bf16 v[184:187], v[44:47], v[88:91], v[184:187]\n"
    "ds_read_b128 v[84:87], v1 offset:16384\n"
    "v_cvt_pk_bf16_f32 v38, v10, v11\n"
    "v_mfma_f32_16x16x32_bf16 v[188:191], v[48:51], v[88:91], v[188:191]\n"
    "v_cvt_pk_bf16_f32 v42, v12, v13\n"
    "v_mfma_f32_16x16x32_bf16 v[192:195], v[44:47], v[92:95], v[192:195]\n"
    "ds_read_b128 v[88:91], v1 offset:18432\n"
    "v_and_b32 v14, s51, v23\n"
    "v_mfma_f32_16x16x32_bf16 v[196:199], v[48:51], v[92:95], v[196:199]\n"
    "v_pk_lshrrev_b16 v15, 12, v23 op_sel_hi:[0,1]\n"
    "v_fma_mix_f32 v16, v14, v28, v29 op_sel_hi:[1,0,0]\n"
    "v_mfma_f32_16x16x32_bf16 v[200:203], v[44:47], v[96:99], v[200:203]\n"
    "ds_read_b128 v[92:95], v1 offset:20480\n"
    "v_fma_mix_f32 v17, v14, v28, v29 op_sel:[1,0,0] op_sel_hi:[1,0,0]\n"
    "v_mfma_f32_16x16x32_bf16 v[204:207], v[48:51], v[96:99], v[204:207]\n"
    "v_fma_mix_f32 v18, v15, v30, v31 op_sel_hi:[1,0,0]\n"
    "v_mfma_f32_16x16x32_bf16 v[208:211], v[44:47], v[100:103], v[208:211]\n"
    "ds_read_b128 v[96:99], v1 offset:22528\n"
    "v_fma_mix_f32 v19, v15, v30, v31 op_sel:[1,0,0] op_sel_hi:[1,0,0]\n"
    "v_mfma_f32_16x16x32_bf16 v[212:215], v[48:51], v[100:103], v[212:215]\n"
    "v_cvt_pk_bf16_f32 v39, v16, v17\n"
    "v_cvt_pk_bf16_f32 v43, v18, v19\n"
    "v_mfma_f32_16x16x32_bf16 v[216:219], v[44:47], v[104:107], v[216:219]\n"
    "s_waitcnt lgkmcnt(3)\n"
    "ds_read_b128 v[100:103], v1 offset:24576\n"
    "v_add_u32 v2, s54, v2\n"
    "v_mfma_f32_16x16x32_bf16 v[220:223], v[48:51], v[104:107], v[220:223]\n"
    "v_add_u32 v4, 2048, v4\n"
    "14:\n"
    "v_mfma_f32_16x16x32_bf16 v[112:115], v[36:39], v[52:55], v[112:115]\n"
    "s_bitcmp1_b32 s57, s53\n"
    "ds_read_b128 v[104:107], v1 offset:26624\n"
    "v_and_b32 v8, s51, v24\n"
    "v_mfma_f32_16x16x32_bf16 v[116:119], v[40:43], v[52:55], v[116:119]\n"
    "s_cselect_b32 s54, s56, s55\n"
    "ds_read2st64_b64 v[20:23], v3 offset0:8 offset1:9\n"
    "v_pk_lshrrev_b16 v9, 12, v24 op_sel_hi:[0,1]\n"
    "v_mfma_f32_16x16x32_bf16 v[120:123], v[36:39], v[56:59], v[120:123]\n"
    "s_sub_u32 s53, s53, 1\n"
    "ds_read_b128 v[52:55], v2 offset:0\n"
    "v_fma_mix_f32 v10, v8, v32, v33 op_sel_hi:[1,0,0]\n"
    "v_mfma_f32_16x16x32_bf16 v[124:127], v[40:43], v[56:59], v[124:127]\n"
    "ds_read_b128 v[28:31], v4 offset:2048\n"
    "v_fma_mix_f32 v11, v8, v32, v33 op_sel:[1,0,0] op_sel_hi:[1,0,0]\n"
    "v_mfma_f32_16x16x32_bf16 v[128:131], v[36:39], v[60:63], v[128:131]\n"
    "ds_read_b128 v[56:59], v2 offset:2048\n"
    "v_fma_mix_f32 v12, v9, v34, v35 op_sel_hi:[1,0,0]\n"
    "v_mfma_f32_16x16x32_bf16 v[132:135], v[40:43], v[60:63], v[132:135]\n"
    "v_fma_mix_f32 v13, v9, v34, v35 op_sel:[1,0,0] op_sel_hi:[1,0,0]\n"
    "v_cvt_pk_bf16_f32 v44, v10, v11\n"
    "v_mfma_f32_16x16x32_bf16 v[136:139], v[36:39], v[64:67], v[136:139]\n"
    "ds_read_b128 v[60:63], v2 offset:4096\n"
    "v_cvt_pk_bf16_f32 v48, v12, v13\n"
    "v_mfma_f32_16x16x32_bf16 v[140:143], v[40:43], v[64:67], v[140:143]\n"
    "v_and_b32 v14, s51, v25\n"
    "v_pk_lshrrev_b16 v15, 12, v25 op_sel_hi:[0,1]\n"
    "v_mfma_f32_16x16x32_bf16 v[144:147], v[36:39], v[68:71], v[144:147]\n"
    "ds_read_b128 v[64:67], v2 offset:6144\n"
    "v_fma_mix_f32 v16, v14, v32, v33 op_sel_hi:[1,0,0]\n"
    "v_mfma_f32_16x16x32_bf16 v[148:151], v[40:43], v[68:71], v[148:151]\n"
    "v_fma_mix_f32 v17, v14, v32, v33 op_sel:[1,0,0] op_sel_hi:[1,0,0]\n"
    "v_mfma_f32_16x16x32_bf16 v[152:155], v[36:39], v[72:75], v[152:155]\n"
    "ds_read_b128 v[68:71], v2 offset:8192\n"
    "v_fma_mix_f32 v18, v15, v34, v35 op_sel_hi:[1,0,0]\n"
    "v_mfma_f32_16x16x32_bf16 v[156:159], v[40:43], v[72:75], v[156:159]\n"
    "v_fma_mix_f32 v19, v15, v34, v35 op_sel:[1,0,0] op_sel_hi:[1,0,0]\n"
    "v_cvt_pk_bf16_f32 v45, v16, v17\n"
    "v_mfma_f32_16x16x32_bf16 v[160:163], v[36:39], v[76:79], v[160:163]\n"
    "ds_read_b128 v[72:75], v2 offset:10240\n"
    "v_cvt_pk_bf16_f32 v49, v18, v19\n"
    "v_mfma_f32_16x16x32_bf16 v[164:167], v[40:43], v[76:79], v[164:167]\n"
    "v_and_b32 v8, s51, v26\n"
    "v_pk_lshrrev_b16 v9, 12, v26 op_sel_hi:[0,1]\n"
    "v_mfma_f32_16x16x32_bf16 v[168:171], v[36:39], v[80:83], v[168:171]\n"
    "ds_read_b128 v[76:79], v2 offset:12288\n"
    "v_fma_mix_f32 v10, v8, v32, v33 op_sel_hi:[1,0,0]\n"
    "v_mfma_f32_16x16x32_bf16 v[172:175], v[40:43], v[80:83], v[172:175]\n"
    "v_fma_mix_f32 v11, v8, v32, v33 op_sel:[1,0,0] op_sel_hi:[1,0,0]\n"
    "v_mfma_f32_16x16x32_bf16 v[176:179], v[36:39], v[84:87], v[176:179]\n"
    "ds_read_b128 v[80:83], v2 offset:14336\n"
    "v_fma_mix_f32 v12, v9, v34, v35 op_sel_hi:[1,0,0]\n"
    "v_mfma_f32_16x16x32_bf16 v[180:183], v[40:43], v[84:87], v[180:183]\n"
    "s_waitcnt lgkmcnt(4)\n"
    "v_fma_mix_f32 v13, v9, v34, v35 op_sel:[1,0,0] op_sel_hi:[1,0,0]\n"
    "v_cvt_pk_bf16_f32 v46, v10, v11\n"
    "v_mfma_f32_16x16x32_bf16 v[184:187], v[36:39], v[88:91], v[184:187]\n"
    "ds_read_b128 v[84:87], v2 offset:16384\n"
    "v_cvt_pk_bf16_f32 v50, v12, v13\n"
    "v_mfma_f32_16x16x32_bf16 v[188:191], v[40:43], v[88:91], v[188:191]\n"
    "v_and_b32 v14, s51, v27\n"
    "v_pk_lshrrev_b16 v15, 12, v27 op_sel_hi:[0,1]\n"
    "v_mfma_f32_16x16x32_bf16 v[192:195], v[36:39], v[92:95], v[192:195]\n"
    "ds_read_b128 v[88:91], v2 offset:18432\n"
    "v_fma_mix_f32 v16, v14, v32, v33 op_sel_hi:[1,0,0]\n"
    "v_mfma_f32_16x16x32_bf16 v[196:199], v[40:43], v[92:95], v[196:199]\n"
    "v_fma_mix_f32 v17, v14, v32, v33 op_sel:[1,0,0] op_sel_hi:[1,0,0]\n"
    "v_mfma_f32_16x16x32_bf16 v[200:203], v[36:39], v[96:99], v[200:203]\n"
    "ds_read_b128 v[92:95], v2 offset:20480\n"
    "v_fma_mix_f32 v18, v15, v34, v35 op_sel_hi:[1,0,0]\n"
    "v_mfma_f32_16x16x32_bf16 v[204:207], v[40:43], v[96:99], v[204:207]\n"
    "v_fma_mix_f32 v19, v15, v34, v35 op_sel:[1,0,0] op_sel_hi:[1,0,0]\n"
    "v_cvt_pk_bf16_f32 v47, v16, v17\n"
    "v_mfma_f32_16x16x32_bf16 v[208:211], v[36:39], v[100:103], v[208:211]\n"
    "ds_read_b128 v[96:99], v2 offset:22528\n"
    "v_cvt_pk_bf16_f32 v51, v18, v19\n"
    "v_mfma_f32_16x16x32_bf16 v[212:215], v[40:43], v[100:103], v[212:215]\n"
    "v_add_u32 v1, s54, v1\n"
    "v_mfma_f32_16x16x32_bf16 v[216:219], v[36:39], v[104:107], v[216:219]\n"
    "ds_read_b128 v[100:103], v2 offset:24576\n"
    "v_add_u32 v3, s54, v3\n"
    "v_mfma_f32_16x16x32_bf16 v[220:223], v[40:43], v[104:107], v[220:223]\n"
    "s_cbranch_scc0 20b\n"
    "v_mfma_f32_16x16x32_bf16 v[112:115], v[44:47], v[52:55], v[112:115]\n"
    "ds_read_b128 v[104:107], v2 offset:26624\n"
    "v_and_b32 v8, s51, v20\n"
    "v_mfma_f32_16x16x32_bf16 v[116:119], v[48:51], v[52:55], v[116:119]\n"
    "v_pk_lshrrev_b16 v9, 12, v20 op_sel_hi:[0,1]\n"
    "v_fma_mix_f32 v10, v8, v28, v29 op_sel_hi:[1,0,0]\n"
    "v_mfma_f32_16x16x32_bf16 v[120:123], v[44:47], v[56:59], v[120:123]\n"
    "v_fma_mix_f32 v11, v8, v28, v29 op_sel:[1,0,0] op_sel_hi:[1,0,0]\n"
    "v_fma_mix_f32 v12, v9, v30, v31 op_sel_hi:[1,0,0]\n"
    "v_mfma_f32_16x16x32_bf16 v[124:127], v[48:51], v[56:59], v[124:127]\n"
    "v_fma_mix_f32 v13, v9, v30, v31 op_sel:[1,0,0] op_sel_hi:[1,0,0]\n"
    "v_cvt_pk_bf16_f32 v36, v10, v11\n"
    "v_mfma_f32_16x16x32_bf16 v[128:131], v[44:47], v[60:63], v[128:131]\n"
    "v_cvt_pk_bf16_f32 v40, v12, v13\n"
    "v_and_b32 v14, s51, v21\n"
    "v_mfma_f32_16x16x32_bf16 v[132:135], v[48:51], v[60:63], v[132:135]\n"
    "s_waitcnt lgkmcnt(0)\n"
    "v_pk_lshrrev_b16 v15, 12, v21 op_sel_hi:[0,1]\n"
    "v_fma_mix_f32 v16, v14, v28, v29 op_sel_hi:[1,0,0]\n"
    "v_mfma_f32_16x16x32_bf16 v[136:139], v[44:47], v[64:67], v[136:139]\n"
    "s_barrier\n"
    "v_fma_mix_f32 v17, v14, v28, v29 op_sel:[1,0,0] op_sel_hi:[1,0,0]\n"
    "v_fma_mix_f32 v18, v15, v30, v31 op_sel_hi:[1,0,0]\n"
    "v_mfma_f32_16x16x32_bf16 v[140:143], v[48:51], v[64:67], v[140:143]\n"
    "v_fma_mix_f32 v19, v15, v30, v31 op_sel:[1,0,0] op_sel_hi:[1,0,0]\n"
    "v_cvt_pk_bf16_f32 v37, v16, v17\n"
    "v_mfma_f32_16x16x32_bf16 v[144:147], v[44:47], v[68:71], v[144:147]\n"
    "v_cvt_pk_bf16_f32 v41, v18, v19\n"
    "v_and_b32 v8, s51, v22\n"
    "v_mfma_f32_16x16x32_bf16 v[148:151], v[48:51], v[68:71], v[148:151]\n"
    "ds_read_b128 v[32:35], v4 offset:3072\n"
    "v_pk_lshrrev_b16 v9, 12, v22 op_sel_hi:[0,1]\n"
    "v_mfma_f32_16x16x32_bf16 v[152:155], v[44:47], v[72:75], v[152:155]\n"
    "ds_read2st64_b64 v[24:27], v254 offset0:76 offset1:77\n"
    "v_fma_mix_f32 v10, v8, v28, v29 op_sel_hi:[1,0,0]\n"
    "v_mfma_f32_16x16x32_bf16 v[156:159], v[48:51], v[72:75], v[156:159]\n"
    "v_fma_mix_f32 v11, v8, v28, v29 op_sel:[1,0,0] op_sel_hi:[1,0,0]\n"
    "v_fma_mix_f32 v12, v9, v30, v31 op_sel_hi:[1,0,0]\n"
    "v_mfma_f32_16x16x32_bf16 v[160:163], v[44:47], v[76:79], v[160:163]\n"
    "v_fma_mix_f32 v13, v9, v30, v31 op_sel:[1,0,0] op_sel_hi:[1,0,0]\n"
    "v_cvt_pk_bf16_f32 v38, v10, v11\n"
    "v_mfma_f32_16x16x32_bf16 v[164:167], v[48:51], v[76:79], v[164:167]\n"
    "v_cvt_pk_bf16_f32 v42, v12, v13\n"
    "v_and_b32 v14, s51, v23\n"
    "v_mfma_f32_16x16x32_bf16 v[168:171], v[44:47], v[80:83], v[168:171]\n"
    "v_pk_lshrrev_b16 v15, 12, v23 op_sel_hi:[0,1]\n"
    "v_fma_mix_f32 v16, v14, v28, v29 op_sel_hi:[1,0,0]\n"
    "v_mfma_f32_16x16x32_bf16 v[172:175], v[48:51], v[80:83], v[172:175]\n"
    "v_fma_mix_f32 v17, v14, v28, v29 op_sel:[1,0,0] op_sel_hi:[1,0,0]\n"
    "v_fma_mix_f32 v18, v15, v30, v31 op_sel_hi:[1,0,0]\n"
    "v_mfma_f32_16x16x32_bf16 v[176:179], v[44:47], v[84:87], v[176:179]\n"
    "v_fma_mix_f32 v19, v15, v30, v31 op_sel:[1,0,0] op_sel_hi:[1,0,0]\n"
    "v_cvt_pk_bf16_f32 v39, v16, v17\n"
    "v_mfma_f32_16x16x32_bf16 v[180:183], v[48:51], v[84:87], v[180:183]\n"
    "v_cvt_pk_bf16_f32 v43, v18, v19\n"
    "v_mfma_f32_16x16x32_bf16 v[184:187], v[44:47], v[88:91], v[184:187]\n"
    "ds_read_b128 v[52:55], v250 offset:38912\n"
    "v_mfma_f32_16x16x32_bf16 v[188:191], v[48:51], v[88:91], v[188:191]\n"
    "s_waitcnt lgkmcnt(1)\n"
    "v_and_b32 v8, s51, v24\n"
    "v_pk_lshrrev_b16 v9, 12, v24 op_sel_hi:[0,1]\n"
    "v_mfma_f32_16x16x32_bf16 v[192:195], v[44:47], v[92:95], v[192:195]\n"
    "ds_read_b128 v[56:59], v250 offset:40960\n"
    "v_fma_mix_f32 v10, v8, v32, v33 op_sel_hi:[1,0,0]\n"
    "v_fma_mix_f32 v11, v8, v32, v33 op_sel:[1,0,0] op_sel_hi:[1,0,0]\n"
    "v_mfma_f32_16x16x32_bf16 v[196:199], v[48:51], v[92:95], v[196:199]\n"
    "v_fma_mix_f32 v12, v9, v34, v35 op_sel_hi:[1,0,0]\n"
    "v_fma_mix_f32 v13, v9, v34, v35 op_sel:[1,0,0] op_sel_hi:[1,0,0]\n"
    "v_mfma_f32_16x16x32_bf16 v[200:203], v[44:47], v[96:99], v[200:203]\n"
    "ds_read_b128 v[60:63], v250 offset:43008\n"
    "v_cvt_pk_bf16_f32 v234, v10, v11\n"
    "v_cvt_pk_bf16_f32 v238, v12, v13\n"
    "v_mfma_f32_16x16x32_bf16 v[204:207], v[48:51], v[96:99], v[204:207]\n"
    "v_and_b32 v14, s51, v25\n"
    "v_pk_lshrrev_b16 v15, 12, v25 op_sel_hi:[0,1]\n"
    "v_mfma_f32_16x16x32_bf16 v[208:211], v[44:47], v[100:103], v[208:211]\n"
    "ds_read_b128 v[64:67], v250 offset:45056\n"
    "v_fma_mix_f32 v16, v14, v32, v33 op_sel_hi:[1,0,0]\n"
    "v_mfma_f32_16x16x32_bf16 v[212:215], v[48:51], v[100:103], v[212:215]\n"
    "v_fma_mix_f32 v17, v14, v32, v33 op_sel:[1,0,0] op_sel_hi:[1,0,0]\n"
    "v_fma_mix_f32 v18, v15, v34, v35 op_sel_hi:[1,0,0]\n"
    "v_mfma_f32_16x16x32_bf16 v[216:219], v[44:47], v[104:107], v[216:219]\n"
    "ds_read_b128 v[68:71], v250 offset:47104\n"
    "v_fma_mix_f32 v19, v15, v34, v35 op_sel:[1,0,0] op_sel_hi:[1,0,0]\n"
    "v_cvt_pk_bf16_f32 v235, v16, v17\n"
    "v_mfma_f32_16x16x32_bf16 v[220:223], v[48:51], v[104:107], v[220:223]\n"
    "s_waitcnt lgkmcnt(4)\n"
    "ds_read_b128 v[28:31], v4 offset:4096\n"
    "v_cvt_pk_bf16_f32 v239, v18, v19\n"
    "v_and_b32 v8, s51, v26\n"
    "v_mfma_f32_16x16x32_bf16 v[112:115], v[36:39], v[52:55], v[112:115]\n"
    "ds_read2st64_b64 v[20:23], v254 offset0:84 offset1:85\n"
    "v_pk_lshrrev_b16 v9, 12, v26 op_sel_hi:[0,1]\n"
    "v_mfma_f32_16x16x32_bf16 v[116:119], v[40:43], v[52:55], v[116:119]\n"
    "s_waitcnt lgkmcnt(5)\n"
    "ds_read_b128 v[72:75], v251 offset:38912\n"
    "v_fma_mix_f32 v10, v8, v32, v33 op_sel_hi:[1,0,0]\n"
    "v_fma_mix_f32 v11, v8, v32, v33 op_sel:[1,0,0] op_sel_hi:[1,0,0]\n"
    "v_mfma_f32_16x16x32_bf16 v[120:123], v[36:39], v[56:59], v[120:123]\n"
    "ds_read_b128 v[76:79], v251 offset:40960\n"
    "v_fma_mix_f32 v12, v9, v34, v35 op_sel_hi:[1,0,0]\n"
    "v_fma_mix_f32 v13, v9, v34, v35 op_sel:[1,0,0] op_sel_hi:[1,0,0]\n"
    "v_mfma_f32_16x16x32_bf16 v[124:127], v[40:43], v[56:59], v[124:127]\n"
    "s_waitcnt lgkmcnt(6)\n"
    "v_cvt_pk_bf16_f32 v236, v10, v11\n"
    "v_cvt_pk_bf16_f32 v240, v12, v13\n"
    "v_mfma_f32_16x16x32_bf16 v[128:131], v[36:39], v[60:63], v[128:131]\n"
    "ds_read_b128 v[80:83], v251 offset:43008\n"
    "v_and_b32 v14, s51, v27\n"
    "v_mfma_f32_16x16x32_bf16 v[132:135], v[40:43], v[60:63], v[132:135]\n"
    "s_waitcnt lgkmcnt(6)\n"
    "v_pk_lshrrev_b16 v15, 12, v27 op_sel_hi:[0,1]\n"
    "v_fma_mix_f32 v16, v14, v32, v33 op_sel_hi:[1,0,0]\n"
    "v_mfma_f32_16x16x32_bf16 v[136:139], v[36:39], v[64:67], v[136:139]\n"
    "ds_read_b128 v[84:87], v251 offset:45056\n"
    "v_fma_mix_f32 v17, v14, v32, v33 op_sel:[1,0,0] op_sel_hi:[1,0,0]\n"
    "v_fma_mix_f32 v18, v15, v34, v35 op_sel_hi:[1,0,0]\n"
    "v_mfma_f32_16x16x32_bf16 v[140:143], v[40:43], v[64:67], v[140:143]\n"
    "s_waitcnt lgkmcnt(5)\n"
    "v_fma_mix_f32 v19, v15, v34, v35 op_sel:[1,0,0] op_sel_hi:[1,0,0]\n"
    "v_cvt_pk_bf16_f32 v237, v16, v17\n"
    "v_mfma_f32_16x16x32_bf16 v[144:147], v[36:39], v[68:71], v[144:147]\n"
    "ds_read_b128 v[88:91], v251 offset:47104\n"
    "v_cvt_pk_bf16_f32 v241, v18, v19\n"
    "v_mfma_f32_16x16x32_bf16 v[148:151], v[40:43], v[68:71], v[148:151]\n"
    "s_waitcnt lgkmcnt(4)\n"
    "v_and_b32 v8, s51, v20\n"
    "v_pk_lshrrev_b16 v9, 12, v20 op_sel_hi:[0,1]\n"
    "v_mfma_f32_16x16x32_bf16 v[112:115], v[234:237], v[72:75], v[112:115]\n"
    "ds_read_b128 v[92:95], v250 offset:49152\n"
    "v_fma_mix_f32 v10, v8, v28, v29 op_sel_hi:[1,0,0]\n"
    "v_fma_mix_f32 v11, v8, v28, v29 op_sel:[1,0,0] op_sel_hi:[1,0,0]\n"
    "v_mfma_f32_16x16x32_bf16 v[116:119], v[238:241], v[72:75], v[116:119]\n"
    "s_waitcnt lgkmcnt(4)\n"
    "v_fma_mix_f32 v12, v9, v30, v31 op_sel_hi:[1,0,0]\n"
    "v_fma_mix_f32 v13, v9, v30, v31 op_sel:[1,0,0] op_sel_hi:[1,0,0]\n"
    "v_mfma_f32_16x16x32_bf16 v[120:123], v[234:237], v[76:79], v[120:123]\n"
    "ds_read_b128 v[52:55], v251 offset:49152\n"
    "v_cvt_pk_bf16_f32 v44, v10, v11\n"
    "v_mfma_f32_16x16x32_bf16 v[124:127], v[238:241], v[76:79], v[124:127]\n"
    "s_waitcnt lgkmcnt(4)\n"
    "v_cvt_pk_bf16_f32 v48, v12, v13\n"
    "v_and_b32 v14, s51, v21\n"
    "v_mfma_f32_16x16x32_bf16 v[128:131], v[234:237], v[80:83], v[128:131]\n"
    "ds_read_b128 v[56:59], v250 offset:51200\n"
    "v_pk_lshrrev_b16 v15, 12, v21 op_sel_hi:[0,1]\n"
    "v_fma_mix_f32 v16, v14, v28, v29 op_sel_hi:[1,0,0]\n"
    "v_mfma_f32_16x16x32_bf16 v[132:135], v[238:241], v[80:83], v[132:135]\n"
    "s_waitcnt lgkmcnt(4)\n"
    "v_fma_mix_f32 v17, v14, v28, v29 op_sel:[1,0,0] op_sel_hi:[1,0,0]\n"
    "v_fma_mix_f32 v18, v15, v30, v31 op_sel_hi:[1,0,0]\n"
    "v_mfma_f32_16x16x32_bf16 v[136:139], v[234:237], v[84:87], v[136:139]\n"
    "ds_read_b128 v[60:63], v251 offset:51200\n"
    "v_fma_mix_f32 v19, v15, v30, v31 op_sel:[1,0,0] op_sel_hi:[1,0,0]\n"
    "v_mfma_f32_16x16x32_bf16 v[140:143], v[238:241], v[84:87], v[140:143]\n"
    "s_waitcnt lgkmcnt(4)\n"
    "v_cvt_pk_bf16_f32 v45, v16, v17\n"
    "v_cvt_pk_bf16_f32 v49, v18, v19\n"
    "v_mfma_f32_16x16x32_bf16 v[144:147], v[234:237], v[88:91], v[144:147]\n"
    "ds_read_b128 v[32:35], v4 offset:5120\n"
    "v_and_b32 v8, s51, v22\n"
    "v_pk_lshrrev_b16 v9, 12, v22 op_sel_hi:[0,1]\n"
    "v_mfma_f32_16x16x32_bf16 v[148:151], v[238:241], v[88:91], v[148:151]\n"
    "s_waitcnt lgkmcnt(4)\n"
    "ds_read_b128 v[64:67], v250 offset:53248\n"
    "v_fma_mix_f32 v10, v8, v28, v29 op_sel_hi:[1,0,0]\n"
    "v_mfma_f32_16x16x32_bf16 v[152:155], v[36:39], v[92:95], v[152:155]\n"
    "v_fma_mix_f32 v11, v8, v28, v29 op_sel:[1,0,0] op_sel_hi:[1,0,0]\n"
    "v_fma_mix_f32 v12, v9, v30, v31 op_sel_hi:[1,0,0]\n"
    "v_mfma_f32_16x16x32_bf16 v[156:159], v[40:43], v[92:95], v[156:159]\n"
    "s_waitcnt lgkmcnt(4)\n"
    "v_fma_mix_f32 v13, v9, v30, v31 op_sel:[1,0,0] op_sel_hi:[1,0,0]\n"
    "v_cvt_pk_bf16_f32 v46, v10, v11\n"
    "v_mfma_f32_16x16x32_bf16 v[152:155], v[234:237], v[52:55], v[152:155]\n"
    "s_barrier\n"
    "ds_read2st64_b64 v[24:27], v254 offset0:152 offset1:153\n"
    "v_cvt_pk_bf16_f32 v50, v12, v13\n"
    "v_and_b32 v14, s51, v23\n"
    "v_mfma_f32_16x16x32_bf16 v[156:159], v[238:241], v[52:55], v[156:159]\n"
    "s_waitcnt lgkmcnt(4)\n"
    "ds_read_b128 v[68:71], v252 offset:12288\n"
    "v_pk_lshrrev_b16 v15, 12, v23 op_sel_hi:[0,1]\n"
    "v_mfma_f32_16x16x32_bf16 v[160:163], v[36:39], v[56:59], v[160:163]\n"
    "ds_read_b128 v[72:75], v252 offset:14336\n"
    "v_fma_mix_f32 v16, v14, v28, v29 op_sel_hi:[1,0,0]\n"
    "v_fma_mix_f32 v17, v14, v28, v29 op_sel:[1,0,0] op_sel_hi:[1,0,0]\n"
    "v_mfma_f32_16x16x32_bf16 v[164:167], v[40:43], v[56:59], v[164:167]\n"
    "s_waitcnt lgkmcnt(5)\n"
    "ds_read_b128 v[76:79], v252 offset:16384\n"
    "v_fma_mix_f32 v18, v15, v30, v31 op_sel_hi:[1,0,0]\n"
    "v_mfma_f32_16x16x32_bf16 v[160:163], v[234:237], v[60:63], v[160:163]\n"
    "ds_read_b128 v[80:83], v252 offset:18432\n"
    "v_fma_mix_f32 v19, v15, v30, v31 op_sel:[1,0,0] op_sel_hi:[1,0,0]\n"
    "v_mfma_f32_16x16x32_bf16 v[164:167], v[238:241], v[60:63], v[164:167]\n"
    "s_waitcnt lgkmcnt(5)\n"
    "v_cvt_pk_bf16_f32 v47, v16, v17\n"
    "v_cvt_pk_bf16_f32 v51, v18, v19\n"
    "v_mfma_f32_16x16x32_bf16 v[168:171], v[36:39], v[64:67], v[168:171]\n"
    "s_waitcnt lgkmcnt(4)\n"
    "ds_read_b128 v[84:87], v252 offset:20480\n"
    "v_and_b32 v8, s51, v24\n"
    "v_pk_lshrrev_b16 v9, 12, v24 op_sel_hi:[0,1]\n"
    "v_mfma_f32_16x16x32_bf16 v[172:175], v[40:43], v[64:67], v[172:175]\n"
    "s_waitcnt lgkmcnt(4)\n"
    "v_fma_mix_f32 v10, v8, v32, v33 op_sel_hi:[1,0,0]\n"
    "v_fma_mix_f32 v11, v8, v32, v33 op_sel:[1,0,0] op_sel_hi:[1,0,0]\n"
    "v_mfma_f32_16x16x32_bf16 v[112:115], v[44:47], v[68:71], v[112:115]\n"
    "ds_read_b128 v[88:91], v252 offset:22528\n"
    "v_fma_mix_f32 v12, v9, v34, v35 op_sel_hi:[1,0,0]\n"
    "v_fma_mix_f32 v13, v9, v34, v35 op_sel:[1,0,0] op_sel_hi:[1,0,0]\n"
    "v_mfma_f32_16x16x32_bf16 v[116:119], v[48:51], v[68:71], v[116:119]\n"
    "s_waitcnt lgkmcnt(4)\n"
    "v_cvt_pk_bf16_f32 v242, v10, v11\n"
    "v_cvt_pk_bf16_f32 v246, v12, v13\n"
    "v_mfma_f32_16x16x32_bf16 v[120:123], v[44:47], v[72:75], v[120:123]\n"
    "ds_read_b128 v[92:95], v252 offset:24576\n"
    "v_and_b32 v14, s51, v25\n"
    "v_mfma_f32_16x16x32_bf16 v[124:127], v[48:51], v[72:75], v[124:127]\n"
    "s_waitcnt lgkmcnt(4)\n"
    "v_pk_lshrrev_b16 v15, 12, v25 op_sel_hi:[0,1]\n"
    "v_fma_mix_f32 v16, v14, v32, v33 op_sel_hi:[1,0,0]\n"
    "v_mfma_f32_16x16x32_bf16 v[128:131], v[44:47], v[76:79], v[128:131]\n"
    "ds_read_b128 v[52:55], v251 offset:53248\n"
    "v_fma_mix_f32 v17, v14, v32, v33 op_sel:[1,0,0] op_sel_hi:[1,0,0]\n"
    "v_fma_mix_f32 v18, v15, v34, v35 op_sel_hi:[1,0,0]\n"
    "v_mfma_f32_16x16x32_bf16 v[132:135], v[48:51], v[76:79], v[132:135]\n"
    "s_waitcnt lgkmcnt(4)\n"
    "v_fma_mix_f32 v19, v15, v34, v35 op_sel:[1,0,0] op_sel_hi:[1,0,0]\n"
    "v_cvt_pk_bf16_f32 v243, v16, v17\n"
    "v_mfma_f32_16x16x32_bf16 v[136:139], v[44:47], v[80:83], v[136:139]\n"
    "ds_read_b128 v[28:31], v4 offset:6144\n"
    "v_cvt_pk_bf16_f32 v247, v18, v19\n"
    "v_and_b32 v8, s51, v26\n"
    "v_mfma_f32_16x16x32_bf16 v[140:143], v[48:51], v[80:83], v[140:143]\n"
    "s_waitcnt lgkmcnt(4)\n"
    "ds_read2st64_b64 v[20:23], v254 offset0:160 offset1:161\n"
    "v_pk_lshrrev_b16 v9, 12, v26 op_sel_hi:[0,1]\n"
    "v_mfma_f32_16x16x32_bf16 v[144:147], v[44:47], v[84:87], v[144:147]\n"
    "ds_read_b128 v[56:59], v252 offset:26624\n"
    "v_fma_mix_f32 v10, v8, v32, v33 op_sel_hi:[1,0,0]\n"
    "v_fma_mix_f32 v11, v8, v32, v33 op_sel:[1,0,0] op_sel_hi:[1,0,0]\n"
    "v_mfma_f32_16x16x32_bf16 v[148:151], v[48:51], v[84:87], v[148:151]\n"
    "s_waitcnt lgkmcnt(5)\n"
    "ds_read_b128 v[60:63], v253 offset:12288\n"
    "v_fma_mix_f32 v12, v9, v34, v35 op_sel_hi:[1,0,0]\n"
    "v_fma_mix_f32 v13, v9, v34, v35 op_sel:[1,0,0] op_sel_hi:[1,0,0]\n"
    "v_mfma_f32_16x16x32_bf16 v[152:155], v[44:47], v[88:91], v[152:155]\n"
    "ds_read_b128 v[64:67], v253 offset:14336\n"
    "v_cvt_pk_bf16_f32 v244, v10, v11\n"
    "v_mfma_f32_16x16x32_bf16 v[156:159], v[48:51], v[88:91], v[156:159]\n"
    "s_waitcnt lgkmcnt(6)\n"
    "v_cvt_pk_bf16_f32 v248, v12, v13\n"
    "v_and_b32 v14, s51, v27\n"
    "v_mfma_f32_16x16x32_bf16 v[160:163], v[44:47], v[92:95], v[160:163]\n"
    "ds_read_b128 v[68:71], v253 offset:16384\n"
    "v_pk_lshrrev_b16 v15, 12, v27 op_sel_hi:[0,1]\n"
    "v_fma_mix_f32 v16, v14, v32, v33 op_sel_hi:[1,0,0]\n"
    "v_mfma_f32_16x16x32_bf16 v[164:167], v[48:51], v[92:95], v[164:167]\n"
    "s_waitcnt lgkmcnt(6)\n"
    "v_fma_mix_f32 v17, v14, v32, v33 op_sel:[1,0,0] op_sel_hi:[1,0,0]\n"
    "v_fma_mix_f32 v18, v15, v34, v35 op_sel_hi:[1,0,0]\n"
    "v_mfma_f32_16x16x32_bf16 v[168:171], v[234:237], v[52:55], v[168:171]\n"
    "ds_read_b128 v[72:75], v253 offset:18432\n"
    "v_fma_mix_f32 v19, v15, v34, v35 op_sel:[1,0,0] op_sel_hi:[1,0,0]\n"
    "v_mfma_f32_16x16x32_bf16 v[172:175], v[238:241], v[52:55], v[172:175]\n"
    "s_waitcnt lgkmcnt(4)\n"
    "v_cvt_pk_bf16_f32 v245, v16, v17\n"
    "v_cvt_pk_bf16_f32 v249, v18, v19\n"
    "v_mfma_f32_16x16x32_bf16 v[168:171], v[44:47], v[56:59], v[168:171]\n"
    "ds_read_b128 v[76:79], v253 offset:20480\n"
    "v_and_b32 v8, s51, v20\n"
    "v_pk_lshrrev_b16 v9, 12, v20 op_sel_hi:[0,1]\n"
    "v_mfma_f32_16x16x32_bf16 v[172:175], v[48:51], v[56:59], v[172:175]\n"
    "s_waitcnt lgkmcnt(4)\n"
    "v_fma_mix_f32 v10, v8, v28, v29 op_sel_hi:[1,0,0]\n"
    "v_fma_mix_f32 v11, v8, v28, v29 op_sel:[1,0,0] op_sel_hi:[1,0,0]\n"
    "v_mfma_f32_16x16x32_bf16 v[112:115], v[242:245], v[60:63], v[112:115]\n"
    "ds_read_b128 v[80:83], v253 offset:22528\n"
    "v_fma_mix_f32 v12, v9, v30, v31 op_sel_hi:[1,0,0]\n"
    "v_mfma_f32_16x16x32_bf16 v[116:119], v[246:249], v[60:63], v[116:119]\n"
    "s_waitcnt lgkmcnt(4)\n"
    "v_fma_mix_f32 v13, v9, v30, v31 op_sel:[1,0,0] op_sel_hi:[1,0,0]\n"
    "v_cvt_pk_bf16_f32 v96, v10, v11\n"
    "v_mfma_f32_16x16x32_bf16 v[120:123], v[242:245], v[64:67], v[120:123]\n"
    "ds_read_b128 v[84:87], v253 offset:24576\n"
    "v_cvt_pk_bf16_f32 v100, v12, v13\n"
    "v_and_b32 v14, s51, v21\n"
    "v_mfma_f32_16x16x32_bf16 v[124:127], v[246:249], v[64:67], v[124:127]\n"
    "s_waitcnt lgkmcnt(4)\n"
    "v_pk_lshrrev_b16 v15, 12, v21 op_sel_hi:[0,1]\n"
    "v_fma_mix_f32 v16, v14, v28, v29 op_sel_hi:[1,0,0]\n"
    "v_mfma_f32_16x16x32_bf16 v[128:131], v[242:245], v[68:71], v[128:131]\n"
    "ds_read_b128 v[88:91], v253 offset:26624\n"
    "v_fma_mix_f32 v17, v14, v28, v29 op_sel:[1,0,0] op_sel_hi:[1,0,0]\n"
    "v_mfma_f32_16x16x32_bf16 v[132:135], v[246:249], v[68:71], v[132:135]\n"
    "s_waitcnt lgkmcnt(4)\n"
    "v_fma_mix_f32 v18, v15, v30, v31 op_sel_hi:[1,0,0]\n"
    "v_fma_mix_f32 v19, v15, v30, v31 op_sel:[1,0,0] op_sel_hi:[1,0,0]\n"
    "v_mfma_f32_16x16x32_bf16 v[136:139], v[242:245], v[72:75], v[136:139]\n"
    "ds_read_b128 v[92:95], v250 offset:55296\n"
    "v_cvt_pk_bf16_f32 v97, v16, v17\n"
    "v_cvt_pk_bf16_f32 v101, v18, v19\n"
    "v_mfma_f32_16x16x32_bf16 v[140:143], v[246:249], v[72:75], v[140:143]\n"
    "s_waitcnt lgkmcnt(4)\n"
    "ds_read_b128 v[32:35], v4 offset:7168\n"
    "v_and_b32 v8, s51, v22\n"
    "v_mfma_f32_16x16x32_bf16 v[144:147], v[242:245], v[76:79], v[144:147]\n"
    "v_pk_lshrrev_b16 v9, 12, v22 op_sel_hi:[0,1]\n"
    "v_fma_mix_f32 v10, v8, v28, v29 op_sel_hi:[1,0,0]\n"
    "v_mfma_f32_16x16x32_bf16 v[148:151], v[246:249], v[76:79], v[148:151]\n"
    "s_waitcnt lgkmcnt(4)\n"
    "v_fma_mix_f32 v11, v8, v28, v29 op_sel:[1,0,0] op_sel_hi:[1,0,0]\n"
    "v_fma_mix_f32 v12, v9, v30, v31 op_sel_hi:[1,0,0]\n"
    "v_mfma_f32_16x16x32_bf16 v[152:155], v[242:245], v[80:83], v[152:155]\n"
    "s_barrier\n"
    "ds_read2st64_b64 v[24:27], v254 offset0:0 offset1:1\n"
    "v_fma_mix_f32 v13, v9, v30, v31 op_sel:[1,0,0] op_sel_hi:[1,0,0]\n"
    "v_cvt_pk_bf16_f32 v98, v10, v11\n"
    "v_mfma_f32_16x16x32_bf16 v[156:159], v[246:249], v[80:83], v[156:159]\n"
    "s_waitcnt lgkmcnt(4)\n"
    "ds_read_b128 v[52:55], v250 offset:0\n"
    "v_cvt_pk_bf16_f32 v102, v12, v13\n"
    "v_mfma_f32_16x16x32_bf16 v[160:163], v[242:245], v[84:87], v[160:163]\n"
    "ds_read_b128 v[56:59], v250 offset:2048\n"
    "v_and_b32 v14, s51, v23\n"
    "v_pk_lshrrev_b16 v15, 12, v23 op_sel_hi:[0,1]\n"
    "v_mfma_f32_16x16x32_bf16 v[164:167], v[246:249], v[84:87], v[164:167]\n"
    "s_waitcnt lgkmcnt(5)\n"
    "ds_read_b128 v[60:63], v250 offset:4096\n"
    "v_fma_mix_f32 v16, v14, v28, v29 op_sel_hi:[1,0,0]\n"
    "v_mfma_f32_16x16x32_bf16 v[168:171], v[242:245], v[88:91], v[168:171]\n"
    "ds_read_b128 v[64:67], v250 offset:6144\n"
    "v_fma_mix_f32 v17, v14, v28, v29 op_sel:[1,0,0] op_sel_hi:[1,0,0]\n"
    "v_fma_mix_f32 v18, v15, v30, v31 op_sel_hi:[1,0,0]\n"
    "v_mfma_f32_16x16x32_bf16 v[172:175], v[246:249], v[88:91], v[172:175]\n"
    "s_waitcnt lgkmcnt(5)\n"
    "v_fma_mix_f32 v19, v15, v30, v31 op_sel:[1,0,0] op_sel_hi:[1,0,0]\n"
    "v_cvt_pk_bf16_f32 v99, v16, v17\n"
    "v_mfma_f32_16x16x32_bf16 v[176:179], v[36:39], v[92:95], v[176:179]\n"
    "ds_read_b128 v[68:71], v250 offset:8192\n"
    "v_cvt_pk_bf16_f32 v103, v18, v19\n"
    "v_mfma_f32_16x16x32_bf16 v[180:183], v[40:43], v[92:95], v[180:183]\n"
    "s_waitcnt lgkmcnt(4)\n"
    "v_and_b32 v8, s51, v24\n"
    "v_pk_lshrrev_b16 v9, 12, v24 op_sel_hi:[0,1]\n"
    "v_fma_mix_f32 v10, v8, v32, v33 op_sel_hi:[1,0,0]\n"
    "v_mfma_f32_16x16x32_bf16 v[112:115], v[96:99], v[52:55], v[112:115]\n"
    "ds_read_b128 v[72:75], v250 offset:10240\n"
    "v_fma_mix_f32 v11, v8, v32, v33 op_sel:[1,0,0] op_sel_hi:[1,0,0]\n"
    "v_fma_mix_f32 v12, v9, v34, v35 op_sel_hi:[1,0,0]\n"
    "v_mfma_f32_16x16x32_bf16 v[116:119], v[100:103], v[52:55], v[116:119]\n"
    "s_waitcnt lgkmcnt(4)\n"
    "v_fma_mix_f32 v13, v9, v34, v35 op_sel:[1,0,0] op_sel_hi:[1,0,0]\n"
    "v_cvt_pk_bf16_f32 v104, v10, v11\n"
    "v_mfma_f32_16x16x32_bf16 v[120:123], v[96:99], v[56:59], v[120:123]\n"
    "ds_read_b128 v[76:79], v250 offset:12288\n"
    "v_cvt_pk_bf16_f32 v108, v12, v13\n"
    "v_and_b32 v14, s51, v25\n"
    "v_pk_lshrrev_b16 v15, 12, v25 op_sel_hi:[0,1]\n"
    "v_mfma_f32_16x16x32_bf16 v[124:127], v[100:103], v[56:59], v[124:127]\n"
    "s_waitcnt lgkmcnt(4)\n"
    "v_fma_mix_f32 v16, v14, v32, v33 op_sel_hi:[1,0,0]\n"
    "v_fma_mix_f32 v17, v14, v32, v33 op_sel:[1,0,0] op_sel_hi:[1,0,0]\n"
    "v_mfma_f32_16x16x32_bf16 v[128:131], v[96:99], v[60:63], v[128:131]\n"
    "ds_read_b128 v[80:83], v251 offset:0\n"
    "v_fma_mix_f32 v18, v15, v34, v35 op_sel_hi:[1,0,0]\n"
    "v_fma_mix_f32 v19, v15, v34, v35 op_sel:[1,0,0] op_sel_hi:[1,0,0]\n"
    "v_mfma_f32_16x16x32_bf16 v[132:135], v[100:103], v[60:63], v[132:135]\n"
    "s_waitcnt lgkmcnt(4)\n"
    "v_cvt_pk_bf16_f32 v105, v16, v17\n"
    "v_cvt_pk_bf16_f32 v109, v18, v19\n"
    "v_mfma_f32_16x16x32_bf16 v[136:139], v[96:99], v[64:67], v[136:139]\n"
    "ds_read_b128 v[84:87], v251 offset:2048\n"
    "v_and_b32 v8, s51, v26\n"
    "v_pk_lshrrev_b16 v9, 12, v26 op_sel_hi:[0,1]\n"
    "v_fma_mix_f32 v10, v8, v32, v33 op_sel_hi:[1,0,0]\n"
    "v_mfma_f32_16x16x32_bf16 v[140:143], v[100:103], v[64:67], v[140:143]\n"
    "s_waitcnt lgkmcnt(4)\n"
    "v_fma_mix_f32 v11, v8, v32, v33 op_sel:[1,0,0] op_sel_hi:[1,0,0]\n"
    "v_fma_mix_f32 v12, v9, v34, v35 op_sel_hi:[1,0,0]\n"
    "v_mfma_f32_16x16x32_bf16 v[144:147], v[96:99], v[68:71], v[144:147]\n"
    "ds_read_b128 v[88:91], v251 offset:4096\n"
    "v_fma_mix_f32 v13, v9, v34, v35 op_sel:[1,0,0] op_sel_hi:[1,0,0]\n"
    "v_cvt_pk_bf16_f32 v106, v10, v11\n"
    "v_mfma_f32_16x16x32_bf16 v[148:151], v[100:103], v[68:71], v[148:151]\n"
    "s_waitcnt lgkmcnt(4)\n"
    "v_cvt_pk_bf16_f32 v110, v12, v13\n"
    "v_and_b32 v14, s51, v27\n"
    "v_pk_lshrrev_b16 v15, 12, v27 op_sel_hi:[0,1]\n"
    "v_mfma_f32_16x16x32_bf16 v[152:155], v[96:99], v[72:75], v[152:155]\n"
    "ds_read_b128 v[92:95], v251 offset:6144\n"
    "v_fma_mix_f32 v16, v14, v32, v33 op_sel_hi:[1,0,0]\n"
    "v_fma_mix_f32 v17, v14, v32, v33 op_sel:[1,0,0] op_sel_hi:[1,0,0]\n"
    "v_mfma_f32_16x16x32_bf16 v[156:159], v[100:103], v[72:75], v[156:159]\n"
    "s_waitcnt lgkmcnt(4)\n"
    "v_fma_mix_f32 v18, v15, v34, v35 op_sel_hi:[1,0,0]\n"
    "v_fma_mix_f32 v19, v15, v34, v35 op_sel:[1,0,0] op_sel_hi:[1,0,0]\n"
    "v_mfma_f32_16x16x32_bf16 v[160:163], v[96:99], v[76:79], v[160:163]\n"
    "ds_read_b128 v[52:55], v251 offset:8192\n"
    "v_cvt_pk_bf16_f32 v107, v16, v17\n"
    "v_cvt_pk_bf16_f32 v111, v18, v19\n"
    "v_mfma_f32_16x16x32_bf16 v[164:167], v[100:103], v[76:79], v[164:167]\n"
    "s_waitcnt lgkmcnt(4)\n"
    "v_mfma_f32_16x16x32_bf16 v[112:115], v[104:107], v[80:83], v[112:115]\n"
    "141:\n"
    "s_cbranch_execz 41b\n"
    "ds_read_b128 v[56:59], v251 offset:10240\n"
    "v_mfma_f32_16x16x32_bf16 v[116:119], v[108:111], v[80:83], v[116:119]\n"
    "s_waitcnt lgkmcnt(4)\n"
    "v_mfma_f32_16x16x32_bf16 v[120:123], v[104:107], v[84:87], v[120:123]\n"
    "ds_read_b128 v[60:63], v251 offset:12288\n"
    "v_mfma_f32_16x16x32_bf16 v[124:127], v[108:111], v[84:87], v[124:127]\n"
    "s_waitcnt lgkmcnt(4)\n"
    "v_mfma_f32_16x16x32_bf16 v[128:131], v[104:107], v[88:91], v[128:131]\n"
    "ds_read_b128 v[64:67], v250 offset:14336\n"
    "s_nop 0\n"
    "v_cvt_pk_bf16_f32 v8, v112, v116\n"
    "v_mfma_f32_16x16x32_bf16 v[132:135], v[108:111], v[88:91], v[132:135]\n"
    "s_waitcnt lgkmcnt(4)\n"
    "v_cvt_pk_bf16_f32 v9, v113, v117\n"
    "v_cvt_pk_bf16_f32 v10, v114, v118\n"
    "v_mfma_f32_16x16x32_bf16 v[136:139], v[104:107], v[92:95], v[136:139]\n"
    "ds_read_b128 v[68:71], v251 offset:14336\n"
    "v_cvt_pk_bf16_f32 v11, v115, v119\n"
    "v_mfma_f32_16x16x32_bf16 v[140:143], v[108:111], v[92:95], v[140:143]\n"
    "s_waitcnt lgkmcnt(4)\n"
    "ds_write_b128 v232, v[8:11] offset:8192\n"
    "v_cvt_pk_bf16_f32 v12, v120, v124\n"
    "v_mfma_f32_16x16x32_bf16 v[144:147], v[104:107], v[52:55], v[144:147]\n"
    "ds_read_b128 v[72:75], v251 offset:55296\n"
    "v_cvt_pk_bf16_f32 v13, v121, v125\n"
    "v_mfma_f32_16x16x32_bf16 v[148:151], v[108:111], v[52:55], v[148:151]\n"
    "s_waitcnt lgkmcnt(5)\n"
    "v_cvt_pk_bf16_f32 v14, v122, v126\n"
    "v_cvt_pk_bf16_f32 v15, v123, v127\n"
    "v_mfma_f32_16x16x32_bf16 v[152:155], v[104:107], v[56:59], v[152:155]\n"
    "ds_read_b128 v[76:79], v252 offset:28672\n"
    "ds_write_b128 v232, v[12:15] offset:12288\n"
    "v_mfma_f32_16x16x32_bf16 v[156:159], v[108:111], v[56:59], v[156:159]\n"
    "s_waitcnt lgkmcnt(6)\n"
    "v_cvt_pk_bf16_f32 v16, v128, v132\n"
    "v_cvt_pk_bf16_f32 v17, v129, v133\n"
    "v_mfma_f32_16x16x32_bf16 v[160:163], v[104:107], v[60:63], v[160:163]\n"
    "ds_read_b128 v[80:83], v253 offset:28672\n"
    "v_cvt_pk_bf16_f32 v18, v130, v134\n"
    "v_mfma_f32_16x16x32_bf16 v[164:167], v[108:111], v[60:63], v[164:167]\n"
    "s_waitcnt lgkmcnt(6)\n"
    "s_waitcnt lgkmcnt(1)\n"
    "s_barrier\n"
    "v_cvt_pk_bf16_f32 v19, v131, v135\n"
    "ds_write_b128 v232, v[16:19] offset:16384\n"
    "v_mfma_f32_16x16x32_bf16 v[168:171], v[96:99], v[64:67], v[168:171]\n"
    "ds_read_b128 v[84:87], v250 offset:16384\n"
    "v_cvt_pk_bf16_f32 v20, v136, v140\n"
    "v_mfma_f32_16x16x32_bf16 v[172:175], v[100:103], v[64:67], v[172:175]\n"
    "v_cvt_pk_bf16_f32 v21, v137, v141\n"
    "v_cvt_pk_bf16_f32 v22, v138, v142\n"
    "v_mfma_f32_16x16x32_bf16 v[168:171], v[104:107], v[68:71], v[168:171]\n"
    "ds_read_b128 v[88:91], v251 offset:16384\n"
    "v_cvt_pk_bf16_f32 v23, v139, v143\n"
    "v_mfma_f32_16x16x32_bf16 v[172:175], v[108:111], v[68:71], v[172:175]\n"
    "ds_write_b128 v232, v[20:23] offset:20480\n"
    "v_cvt_pk_bf16_f32 v24, v144, v148\n"
    "v_mfma_f32_16x16x32_bf16 v[176:179], v[234:237], v[72:75], v[176:179]\n"
    "ds_read_b128 v[92:95], v250 offset:57344\n"
    "v_cvt_pk_bf16_f32 v25, v145, v149\n"
    "v_mfma_f32_16x16x32_bf16 v[180:183], v[238:241], v[72:75], v[180:183]\n"
    "v_cvt_pk_bf16_f32 v26, v146, v150\n"
    "v_cvt_pk_bf16_f32 v27, v147, v151\n"
    "v_mfma_f32_16x16x32_bf16 v[176:179], v[44:47], v[76:79], v[176:179]\n"
    "ds_read_b128 v[52:55], v251 offset:57344\n"
    "ds_write_b128 v232, v[24:27] offset:24576\n"
    "v_mfma_f32_16x16x32_bf16 v[180:183], v[48:51], v[76:79], v[180:183]\n"
    "s_waitcnt lgkmcnt(6)\n"
    "v_cvt_pk_bf16_f32 v28, v152, v156\n"
    "v_cvt_pk_bf16_f32 v29, v153, v157\n"
    "v_mfma_f32_16x16x32_bf16 v[176:179], v[242:245], v[80:83], v[176:179]\n"
    "ds_read_b128 v[56:59], v252 offset:30720\n"
    "v_cvt_pk_bf16_f32 v30, v154, v158\n"
    "v_mfma_f32_16x16x32_bf16 v[180:183], v[246:249], v[80:83], v[180:183]\n"
    "s_waitcnt lgkmcnt(6)\n"
    "v_cvt_pk_bf16_f32 v31, v155, v159\n"
    "ds_write_b128 v232, v[28:31] offset:28672\n"
    "v_mfma_f32_16x16x32_bf16 v[176:179], v[96:99], v[84:87], v[176:179]\n"
    "ds_read_b128 v[60:63], v253 offset:30720\n"
    "v_cvt_pk_bf16_f32 v32, v160, v164\n"
    "v_mfma_f32_16x16x32_bf16 v[180:183], v[100:103], v[84:87], v[180:183]\n"
    "s_waitcnt lgkmcnt(6)\n"
    "v_cvt_pk_bf16_f32 v33, v161, v165\n"
    "v_cvt_pk_bf16_f32 v34, v162, v166\n"
    "v_mfma_f32_16x16x32_bf16 v[176:179], v[104:107], v[88:91], v[176:179]\n"
    "s_waitcnt lgkmcnt(1)\n"
    "s_barrier\n"
    "ds_read_b128 v[64:67], v250 offset:18432\n"
    "v_cvt_pk_bf16_f32 v35, v163, v167\n"
    "v_mfma_f32_16x16x32_bf16 v[180:183], v[108:111], v[88:91], v[180:183]\n"
    "ds_write_b128 v232, v[32:35] offset:40960\n"
    "v_cvt_pk_bf16_f32 v8, v168, v172\n"
    "v_mfma_f32_16x16x32_bf16 v[184:187], v[36:39], v[92:95], v[184:187]\n"
    "ds_read_b128 v[68:71], v251 offset:18432\n"
    "v_cvt_pk_bf16_f32 v9, v169, v173\n"
    "v_mfma_f32_16x16x32_bf16 v[188:191], v[40:43], v[92:95], v[188:191]\n"
    "v_cvt_pk_bf16_f32 v10, v170, v174\n"
    "v_cvt_pk_bf16_f32 v11, v171, v175\n"
    "v_mfma_f32_16x16x32_bf16 v[184:187], v[234:237], v[52:55], v[184:187]\n"
    "ds_read_b128 v[72:75], v250 offset:59392\n"
    "ds_write_b128 v232, v[8:11] offset:32768\n"
    "v_mfma_f32_16x16x32_bf16 v[188:191], v[238:241], v[52:55], v[188:191]\n"
    "v_cvt_pk_bf16_f32 v12, v176, v180\n"
    "v_cvt_pk_bf16_f32 v13, v177, v181\n"
    "v_mfma_f32_16x16x32_bf16 v[184:187], v[44:47], v[56:59], v[184:187]\n"
    "ds_read_b128 v[76:79], v251 offset:59392\n"
    "v_cvt_pk_bf16_f32 v14, v178, v182\n"
    "v_mfma_f32_16x16x32_bf16 v[188:191], v[48:51], v[56:59], v[188:191]\n"
    "s_waitcnt lgkmcnt(6)\n"
    "v_cvt_pk_bf16_f32 v15, v179, v183\n"
    "ds_write_b128 v232, v[12:15] offset:36864\n"
    "v_mfma_f32_16x16x32_bf16 v[184:187], v[242:245], v[60:63], v[184:187]\n"
    "ds_read_b128 v[80:83], v252 offset:32768\n"
    "v_mfma_f32_16x16x32_bf16 v[188:191], v[246:249], v[60:63], v[188:191]\n"
    "s_waitcnt lgkmcnt(6)\n"
    "v_mfma_f32_16x16x32_bf16 v[184:187], v[96:99], v[64:67], v[184:187]\n"
    "ds_read_b128 v[84:87], v253 offset:32768\n"
    "v_mfma_f32_16x16x32_bf16 v[188:191], v[100:103], v[64:67], v[188:191]\n"
    "s_waitcnt lgkmcnt(6)\n"
    "v_mfma_f32_16x16x32_bf16 v[184:187], v[104:107], v[68:71], v[184:187]\n"
    "ds_read_b128 v[88:91], v250 offset:20480\n"
    "v_mfma_f32_16x16x32_bf16 v[188:191], v[108:111], v[68:71], v[188:191]\n"
    "s_waitcnt lgkmcnt(5)\n"
    "v_mfma_f32_16x16x32_bf16 v[192:195], v[36:39], v[72:75], v[192:195]\n"
    "ds_read_b128 v[92:95], v251 offset:20480\n"
    "v_mfma_f32_16x16x32_bf16 v[196:199], v[40:43], v[72:75], v[196:199]\n"
    "s_waitcnt lgkmcnt(4)\n"
    "v_mfma_f32_16x16x32_bf16 v[192:195], v[234:237], v[76:79], v[192:195]\n"
    "ds_read_b128 v[52:55], v250 offset:61440\n"
    "s_nop 0\n"
    "v_cvt_pk_bf16_f32 v16, v184, v188\n"
    "v_mfma_f32_16x16x32_bf16 v[196:199], v[238:241], v[76:79], v[196:199]\n"
    "s_waitcnt lgkmcnt(4)\n"
    "v_cvt_pk_bf16_f32 v17, v185, v189\n"
    "v_cvt_pk_bf16_f32 v18, v186, v190\n"
    "v_mfma_f32_16x16x32_bf16 v[192:195], v[44:47], v[80:83], v[192:195]\n"
    "ds_read_b128 v[56:59], v251 offset:61440\n"
    "v_cvt_pk_bf16_f32 v19, v187, v191\n"
    "v_mfma_f32_16x16x32_bf16 v[196:199], v[48:51], v[80:83], v[196:199]\n"
    "s_waitcnt lgkmcnt(4)\n"
    "ds_write_b128 v232, v[16:19] offset:0\n"
    "v_mfma_f32_16x16x32_bf16 v[192:195], v[242:245], v[84:87], v[192:195]\n"
    "ds_read_b128 v[60:63], v252 offset:34816\n"
    "v_mfma_f32_16x16x32_bf16 v[196:199], v[246:249], v[84:87], v[196:199]\n"
    "s_waitcnt lgkmcnt(5)\n"
    "v_mfma_f32_16x16x32_bf16 v[192:195], v[96:99], v[88:91], v[192:195]\n"
    "s_waitcnt lgkmcnt(1)\n"
    "s_barrier\n"
    "ds_read_b128 v[64:67], v253 offset:34816\n"
    "v_mfma_f32_16x16x32_bf16 v[196:199], v[100:103], v[88:91], v[196:199]\n"
    "v_mfma_f32_16x16x32_bf16 v[192:195], v[104:107], v[92:95], v[192:195]\n"
    "ds_read_b128 v[68:71], v250 offset:22528\n"
    "v_mfma_f32_16x16x32_bf16 v[196:199], v[108:111], v[92:95], v[196:199]\n"
    "v_mfma_f32_16x16x32_bf16 v[200:203], v[36:39], v[52:55], v[200:203]\n"
    "ds_read_b128 v[72:75], v251 offset:22528\n"
    "v_mfma_f32_16x16x32_bf16 v[204:207], v[40:43], v[52:55], v[204:207]\n"
    "v_mfma_f32_16x16x32_bf16 v[200:203], v[234:237], v[56:59], v[200:203]\n"
    "ds_read_b128 v[76:79], v250 offset:63488\n"
    "s_nop 2\n"
    "v_cvt_pk_bf16_f32 v20, v192, v196\n"
    "v_mfma_f32_16x16x32_bf16 v[204:207], v[238:241], v[56:59], v[204:207]\n"
    "s_waitcnt lgkmcnt(4)\n"
    "v_cvt_pk_bf16_f32 v21, v193, v197\n"
    "v_cvt_pk_bf16_f32 v22, v194, v198\n"
    "v_mfma_f32_16x16x32_bf16 v[200:203], v[44:47], v[60:63], v[200:203]\n"
    "ds_read_b128 v[80:83], v251 offset:63488\n"
    "v_cvt_pk_bf16_f32 v23, v195, v199\n"
    "v_mfma_f32_16x16x32_bf16 v[204:207], v[48:51], v[60:63], v[204:207]\n"
    "s_waitcnt lgkmcnt(4)\n"
    "ds_write_b128 v232, v[20:23] offset:4096\n"
    "v_mfma_f32_16x16x32_bf16 v[200:203], v[242:245], v[64:67], v[200:203]\n"
    "ds_read_b128 v[84:87], v252 offset:36864\n"
    "v_mfma_f32_16x16x32_bf16 v[204:207], v[246:249], v[64:67], v[204:207]\n"
    "s_waitcnt lgkmcnt(5)\n"
    "v_mfma_f32_16x16x32_bf16 v[200:203], v[96:99], v[68:71], v[200:203]\n"
    "ds_read_b128 v[88:91], v253 offset:36864\n"
    "v_mfma_f32_16x16x32_bf16 v[204:207], v[100:103], v[68:71], v[204:207]\n"
    "s_waitcnt lgkmcnt(5)\n"
    "v_mfma_f32_16x16x32_bf16 v[200:203], v[104:107], v[72:75], v[200:203]\n"
    "ds_read_b128 v[92:95], v250 offset:24576\n"
    "v_mfma_f32_16x16x32_bf16 v[204:207], v[108:111], v[72:75], v[204:207]\n"
    "s_waitcnt lgkmcnt(5)\n"
    "v_mfma_f32_16x16x32_bf16 v[208:211], v[36:39], v[76:79], v[208:211]\n"
    "ds_read_b128 v[52:55], v251 offset:24576\n"
    "v_mfma_f32_16x16x32_bf16 v[212:215], v[40:43], v[76:79], v[212:215]\n"
    "s_waitcnt lgkmcnt(4)\n"
    "v_mfma_f32_16x16x32_bf16 v[208:211], v[234:237], v[80:83], v[208:211]\n"
    "ds_read_b128 v[56:59], v252 offset:0\n"
    "s_nop 0\n"
    "v_cvt_pk_bf16_f32 v24, v200, v204\n"
    "v_mfma_f32_16x16x32_bf16 v[212:215], v[238:241], v[80:83], v[212:215]\n"
    "s_waitcnt lgkmcnt(4)\n"
    "v_cvt_pk_bf16_f32 v25, v201, v205\n"
    "v_cvt_pk_bf16_f32 v26, v202, v206\n"
    "v_mfma_f32_16x16x32_bf16 v[208:211], v[44:47], v[84:87], v[208:211]\n"
    "ds_read_b128 v[60:63], v253 offset:0\n"
    "v_cvt_pk_bf16_f32 v27, v203, v207\n"
    "v_mfma_f32_16x16x32_bf16 v[212:215], v[48:51], v[84:87], v[212:215]\n"
    "s_waitcnt lgkmcnt(4)\n"
    "ds_write_b128 v233, v[24:27] offset:38912\n"
    "v_mfma_f32_16x16x32_bf16 v[208:211], v[242:245], v[88:91], v[208:211]\n"
    "ds_read_b128 v[64:67], v252 offset:38912\n"
    "v_mfma_f32_16x16x32_bf16 v[212:215], v[246:249], v[88:91], v[212:215]\n"
    "s_waitcnt lgkmcnt(5)\n"
    "v_mfma_f32_16x16x32_bf16 v[208:211], v[96:99], v[92:95], v[208:211]\n"
    "ds_read_b128 v[68:71], v253 offset:38912\n"
    "v_mfma_f32_16x16x32_bf16 v[212:215], v[100:103], v[92:95], v[212:215]\n"
    "s_waitcnt lgkmcnt(5)\n"
    "v_mfma_f32_16x16x32_bf16 v[208:211], v[104:107], v[52:55], v[208:211]\n"
    "ds_read_b128 v[72:75], v250 offset:26624\n"
    "v_mfma_f32_16x16x32_bf16 v[212:215], v[108:111], v[52:55], v[212:215]\n"
    "s_waitcnt lgkmcnt(5)\n"
    "v_mfma_f32_16x16x32_bf16 v[216:219], v[36:39], v[56:59], v[216:219]\n"
    "ds_read_b128 v[76:79], v251 offset:26624\n"
    "v_mfma_f32_16x16x32_bf16 v[220:223], v[40:43], v[56:59], v[220:223]\n"
    "s_waitcnt lgkmcnt(4)\n"
    "v_mfma_f32_16x16x32_bf16 v[216:219], v[234:237], v[60:63], v[216:219]\n"
    "s_nop 1\n"
    "v_cvt_pk_bf16_f32 v28, v208, v212\n"
    "v_cvt_pk_bf16_f32 v29, v209, v213\n"
    "v_mfma_f32_16x16x32_bf16 v[220:223], v[238:241], v[60:63], v[220:223]\n"
    "s_waitcnt lgkmcnt(3)\n"
    "v_cvt_pk_bf16_f32 v30, v210, v214\n"
    "v_cvt_pk_bf16_f32 v31, v211, v215\n"
    "v_mfma_f32_16x16x32_bf16 v[216:219], v[44:47], v[64:67], v[216:219]\n"
    "ds_write_b128 v233, v[28:31] offset:43008\n"
    "v_mfma_f32_16x16x32_bf16 v[220:223], v[48:51], v[64:67], v[220:223]\n"
    "s_waitcnt lgkmcnt(3)\n"
    "v_mfma_f32_16x16x32_bf16 v[216:219], v[242:245], v[68:71], v[216:219]\n"
    "v_mfma_f32_16x16x32_bf16 v[220:223], v[246:249], v[68:71], v[220:223]\n"
    "s_waitcnt lgkmcnt(2)\n"
    "v_mfma_f32_16x16x32_bf16 v[216:219], v[96:99], v[72:75], v[216:219]\n"
    "v_mfma_f32_16x16x32_bf16 v[220:223], v[100:103], v[72:75], v[220:223]\n"
    "s_waitcnt lgkmcnt(1)\n"
    "v_mfma_f32_16x16x32_bf16 v[216:219], v[104:107], v[76:79], v[216:219]\n"
    "v_mfma_f32_16x16x32_bf16 v[220:223], v[108:111], v[76:79], v[220:223]\n"
    "s_nop 7\n"
    "v_cvt_pk_bf16_f32 v32, v216, v220\n"
    "v_cvt_pk_bf16_f32 v33, v217, v221\n"
    "v_cvt_pk_bf16_f32 v34, v218, v222\n"
    "v_cvt_pk_bf16_f32 v35, v219, v223\n"
    "ds_write_b128 v233, v[32:35] offset:0\n"
    "s_waitcnt lgkmcnt(0)\n"
    "s_barrier\n"
    "s_endpgm\n"
    "142:\n"
    "s_branch 41b\n"
    "99:\n"
    :
    : [ka] "s"(__builtin_amdgcn_kernarg_segment_ptr()), [b] "s"(blockIdx.x), [tid] "v"(threadIdx.x), [lb] "s"((unsigned)(size_t)lds)
    : "v1", "v2", "v3", "v4", "v5", "v6", "v7", "v8", "v9", "v10", "v11", "v12", "v13", "v14", "v15", "v16", "v17", "v18", "v19", "v20", "v21", "v22", "v23", "v24", "v25", "v26", "v27", "v28", "v29", "v30", "v31", "v32", "v33", "v34", "v35", "v36", "v37", "v38", "v39", "v40", "v41", "v42", "v43", "v44", "v45", "v46", "v47", "v48", "v49", "v50", "v51", "v52", "v53", "v54", "v55", "v56", "v57", "v58", "v59", "v60", "v61", "v62", "v63", "v64", "v65", "v66", "v67", "v68", "v69", "v70", "v71", "v72", "v73", "v74", "v75", "v76", "v77", "v78", "v79", "v80", "v81", "v82", "v83", "v84", "v85", "v86", "v87", "v88", "v89", "v90", "v91", "v92", "v93", "v94", "v95", "v96", "v97", "v98", "v99", "v100", "v101", "v102", "v103", "v104", "v105", "v106", "v107", "v108", "v109", "v110", "v111", "v112", "v113", "v114", "v115", "v116", "v117", "v118", "v119", "v120", "v121", "v122", "v123", "v124", "v125", "v126", "v127", "v128", "v129", "v130", "v131", "v132", "v133", "v134", "v135", "v136", "v137", "v138", "v139", "v140", "v141", "v142", "v143", "v144", "v145", "v146", "v147", "v148", "v149", "v150", "v151", "v152", "v153", "v154", "v155", "v156", "v157", "v158", "v159", "v160", "v161", "v162", "v163", "v164", "v165", "v166", "v167", "v168", "v169", "v170", "v171", "v172", "v173", "v174", "v175", "v176", "v177", "v178", "v179", "v180", "v181", "v182", "v183", "v184", "v185", "v186", "v187", "v188", "v189", "v190", "v191", "v192", "v193", "v194", "v195", "v196", "v197", "v198", "v199", "v200", "v201", "v202", "v203", "v204", "v205", "v206", "v207", "v208", "v209", "v210", "v211", "v212", "v213", "v214", "v215", "v216", "v217", "v218", "v219", "v220", "v221", "v222", "v223", "v224", "v225", "v226", "v227", "v228", "v229", "v230", "v231", "v232", "v233", "v234", "v235", "v236", "v237", "v238", "v239", "v240", "v241", "v242", "v243", "v244", "v245", "v246", "v247", "v248", "v249", "v250", "v251", "v252", "v253", "v254", "s8", "s9", "s10", "s11", "s12", "s13", "s14", "s15", "s16", "s17", "s18", "s19", "s20", "s21", "s22", "s23", "s24", "s25", "s26", "s27", "s28", "s29", "s30", "s31", "s33", "s34", "s35", "s36", "s37", "s38", "s39", "s40", "s41", "s42", "s43", "s44", "s45", "s46", "s47", "s48", "s49", "s50", "s51", "s52", "s53", "s54", "s55", "s56", "s57", "s58", "s59", "s60", "s61", "s62", "s63", "s64", "s65", "s66", "s67", "s68", "s69", "s70", "s71", "s72", "s73", "s74", "s75", "s76", "s77", "s78", "s79", "scc", "vcc", "memory");

}
extern "C" void kernel_launch(void* const* d_in, const int* in_sizes, int n_in, void* d_out, int out_size, void* d_ws, size_t ws_size, hipStream_t stream) {
  w4a16_kernel<<<256, 512, 0, stream>>>(d_in[2], d_in[3], d_out, d_in[0], d_ws, d_in[1]);
}
```
